```python
import torch

def _softmax(x, axis=-1):
    return torch.softmax(x, dim=axis)
def _gelu(x):
    return torch.nn.functional.gelu(x, approximate="tanh")
def _silu(x):
    return torch.nn.functional.silu(x)
def _sigmoid(x):
    return torch.sigmoid(x)
def _layernorm(x, axis=-1, eps=1e-5):
    m = x.mean(dim=axis, keepdim=True)
    v = x.var(dim=axis, keepdim=True, unbiased=False)
    return (x - m) / torch.sqrt(v + eps)
def _scatter_add(tgt, idx, src, axis=0):
    return tgt.clone().index_add_(axis, idx, src)
def _segment_sum(src, seg, n):
    out = torch.zeros((n,) + src.shape[1:], dtype=src.dtype)
    return out.index_add_(0, seg, src)
def _pad(x, pw):
    flat = tuple(v for pair in reversed(pw) for v in pair)
    return torch.nn.functional.pad(x, flat)

kernel_name = 'standalone__gqa_softmax__prompt'

def setup_inputs():

    _w = (lambda a: (lambda t: t.to(torch.bfloat16) if t.is_floating_point() else t)(torch.as_tensor(a)))
    B, Hq, Hkv, Sq, Sk, D = 1, 32, 32, 2048, 2048, 128
    return {
        "q": _w((lambda *s: torch.randn(*s, dtype=torch.bfloat16) * 0.8)(B, Hq, Sq, D)),
        "k": _w((lambda *s: torch.randn(*s, dtype=torch.bfloat16) * 0.8)(B, Hkv, Sk, D)),
        "v": _w((lambda *s: torch.randn(*s, dtype=torch.bfloat16) * 0.05)(B, Hkv, Sk, D)),
    }

def reference(q, k, v):
    B, Hq, Sq, D = q.shape
    Hkv = k.shape[1]
    rep = Hq // Hkv
    k = (lambda x, n, axis: torch.repeat_interleave(x, n, dim=axis))(k, rep, axis=1)
    v = (lambda x, n, axis: torch.repeat_interleave(x, n, dim=axis))(v, rep, axis=1)
    scale = D ** -0.5
    scores = torch.einsum("bhqd,bhkd->bhqk", q, k) * scale
    p = _softmax(scores, axis=-1)
    return torch.einsum("bhqk,bhkd->bhqd", p, v)
```

```cpp
#include <hip/hip_runtime.h>
typedef unsigned u32x4 __attribute__((ext_vector_type(4)));
#define SRD_WORD3 0x00020000
extern "C" __global__ __launch_bounds__(256, 1)
void attn_fwd_asm(const unsigned short* __restrict__ Qb, const unsigned short* __restrict__ Kb, const unsigned short* __restrict__ Vb, unsigned short* __restrict__ O) {
  __shared__ __attribute__((aligned(16))) char lds[131072];
  const unsigned tid = threadIdx.x, lane = tid & 63u, wid = __builtin_amdgcn_readfirstlane(tid >> 6), l32 = lane & 31u, hi = lane >> 5;
  const unsigned blk = blockIdx.x, h = (blk & 7u) * 4u + ((blk >> 6) & 3u), y = (blk >> 3) & 7u;
  const unsigned ldsb = (unsigned)(size_t)lds;
  const size_t hoff = (size_t)h * 2048u * 256u;
  __amdgpu_buffer_rsrc_t srdQ = __builtin_amdgcn_make_buffer_rsrc((char*)Qb + hoff, (short)0, 2048 * 256, SRD_WORD3);
  __amdgpu_buffer_rsrc_t srdK = __builtin_amdgcn_make_buffer_rsrc((char*)Kb + hoff, (short)0, 2048 * 256, SRD_WORD3);
  __amdgpu_buffer_rsrc_t srdV = __builtin_amdgcn_make_buffer_rsrc((char*)Vb + hoff, (short)0, 2048 * 256, SRD_WORD3);
  __amdgpu_buffer_rsrc_t srdO = __builtin_amdgcn_make_buffer_rsrc((char*)O + hoff, (short)0, 2048 * 256, SRD_WORD3);
  unsigned vkb[4];
  for (int j = 0; j < 4; ++j) vkb[j] = ldsb + 256u * l32 + 16u * ((2u * j + hi) ^ (l32 & 7u));
  const unsigned vvb = ldsb + 0x8000u + (((lane & 3u) << 3) | (((lane >> 2) & 3u) << 6) | (((lane >> 4) & 1u) << 5) | (hi << 8));
  const unsigned vks0 = 256u * (lane >> 4) + 16u * ((lane & 15u) ^ (lane >> 4)), vks1 = 256u * (lane >> 4) + 16u * ((lane & 15u) ^ (4u + (lane >> 4)));
  const unsigned vvs = 256u * ((lane >> 2) & 7u) + 64u * hi + 16u * (lane & 3u);
  const unsigned vkg = 256u * l32 + 16u * hi;
  const unsigned m0k = ldsb + 4096u * wid, m0v = ldsb + 0x8000u + 4096u * wid, m0q = ldsb + 0x10000u + 16384u * wid;
  const unsigned sofk = 4096u * wid, sofq = (256u * y + 64u * wid) * 256u, qoff = 0x10000u + 16384u * wid;
  const unsigned skbias = 3u * 16384u + sofk;
  const unsigned ostg = ldsb + 0x10000u + 16384u * wid;
  const unsigned vol15 = 16u * (l32 & 15u), vorow = ostg + 256u * l32 + 8u * hi;
  const unsigned vord = ostg + 256u * (lane >> 4) + 16u * ((lane & 15u) ^ ((lane >> 4) & 15u));
  const unsigned vost = 16u * lane;

  asm volatile(
    "v_mov_b32_e32 v249, %[vlane]\n\t"
    "v_and_b32_e32 v0, 31, v249\n\t"
    "v_lshrrev_b32_e32 v1, 5, v249\n\t"
    "v_and_b32_e32 v2, 7, v249\n\t"
    "v_lshlrev_b32_e32 v3, 8, v0\n\t"
    "v_add_u32_e32 v3, %[ldsb], v3\n\t"
    "v_add_u32_e32 v4, 0, v1\n\t"
    "v_xor_b32_e32 v4, v4, v2\n\t"
    "v_lshl_add_u32 v224, v4, 4, v3\n\t"
    "v_add_u32_e32 v4, 2, v1\n\t"
    "v_xor_b32_e32 v4, v4, v2\n\t"
    "v_lshl_add_u32 v225, v4, 4, v3\n\t"
    "v_add_u32_e32 v4, 4, v1\n\t"
    "v_xor_b32_e32 v4, v4, v2\n\t"
    "v_lshl_add_u32 v226, v4, 4, v3\n\t"
    "v_add_u32_e32 v4, 6, v1\n\t"
    "v_xor_b32_e32 v4, v4, v2\n\t"
    "v_lshl_add_u32 v227, v4, 4, v3\n\t"
    "v_and_b32_e32 v4, 3, v249\n\t"
    "v_lshlrev_b32_e32 v5, 3, v4\n\t"
    "v_bfe_u32 v6, v249, 2, 2\n\t"
    "v_lshl_or_b32 v5, v6, 6, v5\n\t"
    "v_bfe_u32 v6, v249, 4, 1\n\t"
    "v_lshl_or_b32 v5, v6, 5, v5\n\t"
    "v_lshl_or_b32 v5, v1, 8, v5\n\t"
    "v_add_u32_e32 v5, 0x8000, v5\n\t"
    "v_add_u32_e32 v228, %[ldsb], v5\n\t"
    "v_lshrrev_b32_e32 v6, 4, v249\n\t"
    "v_and_b32_e32 v7, 15, v249\n\t"
    "v_xor_b32_e32 v8, v7, v6\n\t"
    "v_lshlrev_b32_e32 v9, 8, v6\n\t"
    "v_lshl_add_u32 v229, v8, 4, v9\n\t"
    "v_xor_b32_e32 v8, 4, v8\n\t"
    "v_lshl_add_u32 v230, v8, 4, v9\n\t"
    "v_bfe_u32 v6, v249, 2, 3\n\t"
    "v_lshlrev_b32_e32 v6, 8, v6\n\t"
    "v_lshl_add_u32 v6, v1, 6, v6\n\t"
    "v_lshl_add_u32 v231, v4, 4, v6\n\t"
    "v_lshlrev_b32_e32 v6, 8, v0\n\t"
    "v_lshl_add_u32 v248, v1, 4, v6\n\t"
    "s_mov_b32 s54, 0x47800000\n\t"
    "s_add_i32 s40, %[m0k], 0\n\t"
    "s_add_i32 s42, %[m0v], 0\n\t"
    "s_add_i32 s43, %[m0v], 1024\n\t"
    "s_add_i32 s44, %[m0v], 2048\n\t"
    "s_add_i32 s45, %[m0v], 3072\n\t"
    "s_add_i32 s41, %[m0k], 16384\n\t"
    "s_add_i32 s46, %[m0v], 16384\n\t"
    "s_add_i32 s47, %[m0v], 17408\n\t"
    "s_add_i32 s48, %[m0v], 18432\n\t"
    "s_add_i32 s49, %[m0v], 19456\n\t"
    "s_add_i32 s52, %[m0q], 0\n\t"
    "s_mov_b32 m0, s52\n\t"
    "s_add_i32 s52, %[sofq], 0\n\t"
    "buffer_load_dwordx4 v229, %[srdQ], s52 offen lds\n\t"
    "s_add_i32 s52, %[m0q], 1024\n\t"
    "s_mov_b32 m0, s52\n\t"
    "s_add_i32 s52, %[sofq], 1024\n\t"
    "buffer_load_dwordx4 v230, %[srdQ], s52 offen lds\n\t"
    "s_add_i32 s52, %[m0q], 2048\n\t"
    "s_mov_b32 m0, s52\n\t"
    "s_add_i32 s52, %[sofq], 2048\n\t"
    "buffer_load_dwordx4 v229, %[srdQ], s52 offen lds\n\t"
    "s_add_i32 s52, %[m0q], 3072\n\t"
    "s_mov_b32 m0, s52\n\t"
    "s_add_i32 s52, %[sofq], 3072\n\t"
    "buffer_load_dwordx4 v230, %[srdQ], s52 offen lds\n\t"
    "s_add_i32 s52, %[m0q], 4096\n\t"
    "s_mov_b32 m0, s52\n\t"
    "s_add_i32 s52, %[sofq], 4096\n\t"
    "buffer_load_dwordx4 v229, %[srdQ], s52 offen lds\n\t"
    "s_add_i32 s52, %[m0q], 5120\n\t"
    "s_mov_b32 m0, s52\n\t"
    "s_add_i32 s52, %[sofq], 5120\n\t"
    "buffer_load_dwordx4 v230, %[srdQ], s52 offen lds\n\t"
    "s_add_i32 s52, %[m0q], 6144\n\t"
    "s_mov_b32 m0, s52\n\t"
    "s_add_i32 s52, %[sofq], 6144\n\t"
    "buffer_load_dwordx4 v229, %[srdQ], s52 offen lds\n\t"
    "s_add_i32 s52, %[m0q], 7168\n\t"
    "s_mov_b32 m0, s52\n\t"
    "s_add_i32 s52, %[sofq], 7168\n\t"
    "buffer_load_dwordx4 v230, %[srdQ], s52 offen lds\n\t"
    "s_add_i32 s52, %[m0q], 8192\n\t"
    "s_mov_b32 m0, s52\n\t"
    "s_add_i32 s52, %[sofq], 8192\n\t"
    "buffer_load_dwordx4 v229, %[srdQ], s52 offen lds\n\t"
    "s_add_i32 s52, %[m0q], 9216\n\t"
    "s_mov_b32 m0, s52\n\t"
    "s_add_i32 s52, %[sofq], 9216\n\t"
    "buffer_load_dwordx4 v230, %[srdQ], s52 offen lds\n\t"
    "s_add_i32 s52, %[m0q], 10240\n\t"
    "s_mov_b32 m0, s52\n\t"
    "s_add_i32 s52, %[sofq], 10240\n\t"
    "buffer_load_dwordx4 v229, %[srdQ], s52 offen lds\n\t"
    "s_add_i32 s52, %[m0q], 11264\n\t"
    "s_mov_b32 m0, s52\n\t"
    "s_add_i32 s52, %[sofq], 11264\n\t"
    "buffer_load_dwordx4 v230, %[srdQ], s52 offen lds\n\t"
    "s_add_i32 s52, %[m0q], 12288\n\t"
    "s_mov_b32 m0, s52\n\t"
    "s_add_i32 s52, %[sofq], 12288\n\t"
    "buffer_load_dwordx4 v229, %[srdQ], s52 offen lds\n\t"
    "s_add_i32 s52, %[m0q], 13312\n\t"
    "s_mov_b32 m0, s52\n\t"
    "s_add_i32 s52, %[sofq], 13312\n\t"
    "buffer_load_dwordx4 v230, %[srdQ], s52 offen lds\n\t"
    "s_add_i32 s52, %[m0q], 14336\n\t"
    "s_mov_b32 m0, s52\n\t"
    "s_add_i32 s52, %[sofq], 14336\n\t"
    "buffer_load_dwordx4 v229, %[srdQ], s52 offen lds\n\t"
    "s_add_i32 s52, %[m0q], 15360\n\t"
    "s_mov_b32 m0, s52\n\t"
    "s_add_i32 s52, %[sofq], 15360\n\t"
    "buffer_load_dwordx4 v230, %[srdQ], s52 offen lds\n\t"
    "s_mov_b32 s50, %[sofk]\n\t"
    "s_mov_b32 s51, %[sofk]\n\t"
    "s_mov_b32 m0, s40\n\t"
    "buffer_load_dwordx4 v229, %[srdK], s50 offen offset:0 lds\n\t"
    "buffer_load_dwordx4 v230, %[srdK], s50 offen offset:1024 lds\n\t"
    "buffer_load_dwordx4 v229, %[srdK], s50 offen offset:2048 lds\n\t"
    "buffer_load_dwordx4 v230, %[srdK], s50 offen offset:3072 lds\n\t"
    "s_mov_b32 m0, s42\n\t"
    "buffer_load_dwordx4 v231, %[srdV], s51 offen lds\n\t"
    "s_mov_b32 m0, s43\n\t"
    "s_add_i32 s52, s51, 128\n\t"
    "buffer_load_dwordx4 v231, %[srdV], s52 offen lds\n\t"
    "s_mov_b32 m0, s44\n\t"
    "s_add_i32 s52, s51, 2048\n\t"
    "buffer_load_dwordx4 v231, %[srdV], s52 offen lds\n\t"
    "s_mov_b32 m0, s45\n\t"
    "s_add_i32 s52, s51, 2176\n\t"
    "buffer_load_dwordx4 v231, %[srdV], s52 offen lds\n\t"
    "s_add_i32 s50, s50, 16384\n\t"
    "s_mov_b32 m0, s41\n\t"
    "buffer_load_dwordx4 v229, %[srdK], s50 offen offset:0 lds\n\t"
    "buffer_load_dwordx4 v230, %[srdK], s50 offen offset:1024 lds\n\t"
    "buffer_load_dwordx4 v229, %[srdK], s50 offen offset:2048 lds\n\t"
    "buffer_load_dwordx4 v230, %[srdK], s50 offen offset:3072 lds\n\t"
    "s_add_i32 s50, s50, 16384\n\t"
    "s_add_i32 s51, s51, 16384\n\t"
    "v_accvgpr_write_b32 a0, 0\n\t"
    "v_accvgpr_write_b32 a1, 0\n\t"
    "v_accvgpr_write_b32 a2, 0\n\t"
    "v_accvgpr_write_b32 a3, 0\n\t"
    "v_accvgpr_write_b32 a4, 0\n\t"
    "v_accvgpr_write_b32 a5, 0\n\t"
    "v_accvgpr_write_b32 a6, 0\n\t"
    "v_accvgpr_write_b32 a7, 0\n\t"
    "v_accvgpr_write_b32 a8, 0\n\t"
    "v_accvgpr_write_b32 a9, 0\n\t"
    "v_accvgpr_write_b32 a10, 0\n\t"
    "v_accvgpr_write_b32 a11, 0\n\t"
    "v_accvgpr_write_b32 a12, 0\n\t"
    "v_accvgpr_write_b32 a13, 0\n\t"
    "v_accvgpr_write_b32 a14, 0\n\t"
    "v_accvgpr_write_b32 a15, 0\n\t"
    "v_accvgpr_write_b32 a16, 0\n\t"
    "v_accvgpr_write_b32 a17, 0\n\t"
    "v_accvgpr_write_b32 a18, 0\n\t"
    "v_accvgpr_write_b32 a19, 0\n\t"
    "v_accvgpr_write_b32 a20, 0\n\t"
    "v_accvgpr_write_b32 a21, 0\n\t"
    "v_accvgpr_write_b32 a22, 0\n\t"
    "v_accvgpr_write_b32 a23, 0\n\t"
    "v_accvgpr_write_b32 a24, 0\n\t"
    "v_accvgpr_write_b32 a25, 0\n\t"
    "v_accvgpr_write_b32 a26, 0\n\t"
    "v_accvgpr_write_b32 a27, 0\n\t"
    "v_accvgpr_write_b32 a28, 0\n\t"
    "v_accvgpr_write_b32 a29, 0\n\t"
    "v_accvgpr_write_b32 a30, 0\n\t"
    "v_accvgpr_write_b32 a31, 0\n\t"
    "v_accvgpr_write_b32 a32, 0\n\t"
    "v_accvgpr_write_b32 a33, 0\n\t"
    "v_accvgpr_write_b32 a34, 0\n\t"
    "v_accvgpr_write_b32 a35, 0\n\t"
    "v_accvgpr_write_b32 a36, 0\n\t"
    "v_accvgpr_write_b32 a37, 0\n\t"
    "v_accvgpr_write_b32 a38, 0\n\t"
    "v_accvgpr_write_b32 a39, 0\n\t"
    "v_accvgpr_write_b32 a40, 0\n\t"
    "v_accvgpr_write_b32 a41, 0\n\t"
    "v_accvgpr_write_b32 a42, 0\n\t"
    "v_accvgpr_write_b32 a43, 0\n\t"
    "v_accvgpr_write_b32 a44, 0\n\t"
    "v_accvgpr_write_b32 a45, 0\n\t"
    "v_accvgpr_write_b32 a46, 0\n\t"
    "v_accvgpr_write_b32 a47, 0\n\t"
    "v_accvgpr_write_b32 a48, 0\n\t"
    "v_accvgpr_write_b32 a49, 0\n\t"
    "v_accvgpr_write_b32 a50, 0\n\t"
    "v_accvgpr_write_b32 a51, 0\n\t"
    "v_accvgpr_write_b32 a52, 0\n\t"
    "v_accvgpr_write_b32 a53, 0\n\t"
    "v_accvgpr_write_b32 a54, 0\n\t"
    "v_accvgpr_write_b32 a55, 0\n\t"
    "v_accvgpr_write_b32 a56, 0\n\t"
    "v_accvgpr_write_b32 a57, 0\n\t"
    "v_accvgpr_write_b32 a58, 0\n\t"
    "v_accvgpr_write_b32 a59, 0\n\t"
    "v_accvgpr_write_b32 a60, 0\n\t"
    "v_accvgpr_write_b32 a61, 0\n\t"
    "v_accvgpr_write_b32 a62, 0\n\t"
    "v_accvgpr_write_b32 a63, 0\n\t"
    "v_accvgpr_write_b32 a64, 0\n\t"
    "v_accvgpr_write_b32 a65, 0\n\t"
    "v_accvgpr_write_b32 a66, 0\n\t"
    "v_accvgpr_write_b32 a67, 0\n\t"
    "v_accvgpr_write_b32 a68, 0\n\t"
    "v_accvgpr_write_b32 a69, 0\n\t"
    "v_accvgpr_write_b32 a70, 0\n\t"
    "v_accvgpr_write_b32 a71, 0\n\t"
    "v_accvgpr_write_b32 a72, 0\n\t"
    "v_accvgpr_write_b32 a73, 0\n\t"
    "v_accvgpr_write_b32 a74, 0\n\t"
    "v_accvgpr_write_b32 a75, 0\n\t"
    "v_accvgpr_write_b32 a76, 0\n\t"
    "v_accvgpr_write_b32 a77, 0\n\t"
    "v_accvgpr_write_b32 a78, 0\n\t"
    "v_accvgpr_write_b32 a79, 0\n\t"
    "v_accvgpr_write_b32 a80, 0\n\t"
    "v_accvgpr_write_b32 a81, 0\n\t"
    "v_accvgpr_write_b32 a82, 0\n\t"
    "v_accvgpr_write_b32 a83, 0\n\t"
    "v_accvgpr_write_b32 a84, 0\n\t"
    "v_accvgpr_write_b32 a85, 0\n\t"
    "v_accvgpr_write_b32 a86, 0\n\t"
    "v_accvgpr_write_b32 a87, 0\n\t"
    "v_accvgpr_write_b32 a88, 0\n\t"
    "v_accvgpr_write_b32 a89, 0\n\t"
    "v_accvgpr_write_b32 a90, 0\n\t"
    "v_accvgpr_write_b32 a91, 0\n\t"
    "v_accvgpr_write_b32 a92, 0\n\t"
    "v_accvgpr_write_b32 a93, 0\n\t"
    "v_accvgpr_write_b32 a94, 0\n\t"
    "v_accvgpr_write_b32 a95, 0\n\t"
    "v_accvgpr_write_b32 a96, 0\n\t"
    "v_accvgpr_write_b32 a97, 0\n\t"
    "v_accvgpr_write_b32 a98, 0\n\t"
    "v_accvgpr_write_b32 a99, 0\n\t"
    "v_accvgpr_write_b32 a100, 0\n\t"
    "v_accvgpr_write_b32 a101, 0\n\t"
    "v_accvgpr_write_b32 a102, 0\n\t"
    "v_accvgpr_write_b32 a103, 0\n\t"
    "v_accvgpr_write_b32 a104, 0\n\t"
    "v_accvgpr_write_b32 a105, 0\n\t"
    "v_accvgpr_write_b32 a106, 0\n\t"
    "v_accvgpr_write_b32 a107, 0\n\t"
    "v_accvgpr_write_b32 a108, 0\n\t"
    "v_accvgpr_write_b32 a109, 0\n\t"
    "v_accvgpr_write_b32 a110, 0\n\t"
    "v_accvgpr_write_b32 a111, 0\n\t"
    "v_accvgpr_write_b32 a112, 0\n\t"
    "v_accvgpr_write_b32 a113, 0\n\t"
    "v_accvgpr_write_b32 a114, 0\n\t"
    "v_accvgpr_write_b32 a115, 0\n\t"
    "v_accvgpr_write_b32 a116, 0\n\t"
    "v_accvgpr_write_b32 a117, 0\n\t"
    "v_accvgpr_write_b32 a118, 0\n\t"
    "v_accvgpr_write_b32 a119, 0\n\t"
    "v_accvgpr_write_b32 a120, 0\n\t"
    "v_accvgpr_write_b32 a121, 0\n\t"
    "v_accvgpr_write_b32 a122, 0\n\t"
    "v_accvgpr_write_b32 a123, 0\n\t"
    "v_accvgpr_write_b32 a124, 0\n\t"
    "v_accvgpr_write_b32 a125, 0\n\t"
    "v_accvgpr_write_b32 a126, 0\n\t"
    "v_accvgpr_write_b32 a127, 0\n\t"
    "v_mov_b32_e32 v236, 0\n\t"
    "v_mov_b32_e32 v237, 0\n\t"
    "v_mov_b32_e32 v238, 0\n\t"
    "v_mov_b32_e32 v239, 0\n\t"
    "v_add_u32_e32 v200, %[qoff], v224\n\t"
    "v_add_u32_e32 v201, %[qoff], v225\n\t"
    "v_add_u32_e32 v202, %[qoff], v226\n\t"
    "v_add_u32_e32 v203, %[qoff], v227\n\t"
    "s_waitcnt vmcnt(12)\n\t"
    "ds_read_b128 v[0:3], v200 offset:0\n\t"
    "ds_read_b128 v[4:7], v201 offset:0\n\t"
    "ds_read_b128 v[8:11], v202 offset:0\n\t"
    "ds_read_b128 v[12:15], v203 offset:0\n\t"
    "ds_read_b128 v[16:19], v200 offset:128\n\t"
    "ds_read_b128 v[20:23], v201 offset:128\n\t"
    "ds_read_b128 v[24:27], v202 offset:128\n\t"
    "ds_read_b128 v[28:31], v203 offset:128\n\t"
    "ds_read_b128 v[32:35], v200 offset:8192\n\t"
    "ds_read_b128 v[36:39], v201 offset:8192\n\t"
    "ds_read_b128 v[40:43], v202 offset:8192\n\t"
    "ds_read_b128 v[44:47], v203 offset:8192\n\t"
    "ds_read_b128 v[48:51], v200 offset:8320\n\t"
    "ds_read_b128 v[52:55], v201 offset:8320\n\t"
    "ds_read_b128 v[56:59], v202 offset:8320\n\t"
    "ds_read_b128 v[60:63], v203 offset:8320\n\t"
    "s_waitcnt lgkmcnt(0)\n\t"
    "v_lshlrev_b32_e32 v242, 16, v0\n\t"
    "v_and_b32_e32 v243, 0xffff0000, v0\n\t"
    "v_mul_f32_e32 v242, 0x3e0293ee, v242\n\t"
    "v_mul_f32_e32 v243, 0x3e0293ee, v243\n\t"
    "v_cvt_pk_bf16_f32 v242, v242, v243\n\t"
    "v_accvgpr_write_b32 a128, v242\n\t"
    "v_lshlrev_b32_e32 v242, 16, v1\n\t"
    "v_and_b32_e32 v243, 0xffff0000, v1\n\t"
    "v_mul_f32_e32 v242, 0x3e0293ee, v242\n\t"
    "v_mul_f32_e32 v243, 0x3e0293ee, v243\n\t"
    "v_cvt_pk_bf16_f32 v242, v242, v243\n\t"
    "v_accvgpr_write_b32 a129, v242\n\t"
    "v_lshlrev_b32_e32 v242, 16, v2\n\t"
    "v_and_b32_e32 v243, 0xffff0000, v2\n\t"
    "v_mul_f32_e32 v242, 0x3e0293ee, v242\n\t"
    "v_mul_f32_e32 v243, 0x3e0293ee, v243\n\t"
    "v_cvt_pk_bf16_f32 v242, v242, v243\n\t"
    "v_accvgpr_write_b32 a130, v242\n\t"
    "v_lshlrev_b32_e32 v242, 16, v3\n\t"
    "v_and_b32_e32 v243, 0xffff0000, v3\n\t"
    "v_mul_f32_e32 v242, 0x3e0293ee, v242\n\t"
    "v_mul_f32_e32 v243, 0x3e0293ee, v243\n\t"
    "v_cvt_pk_bf16_f32 v242, v242, v243\n\t"
    "v_accvgpr_write_b32 a131, v242\n\t"
    "v_lshlrev_b32_e32 v242, 16, v4\n\t"
    "v_and_b32_e32 v243, 0xffff0000, v4\n\t"
    "v_mul_f32_e32 v242, 0x3e0293ee, v242\n\t"
    "v_mul_f32_e32 v243, 0x3e0293ee, v243\n\t"
    "v_cvt_pk_bf16_f32 v242, v242, v243\n\t"
    "v_accvgpr_write_b32 a132, v242\n\t"
    "v_lshlrev_b32_e32 v242, 16, v5\n\t"
    "v_and_b32_e32 v243, 0xffff0000, v5\n\t"
    "v_mul_f32_e32 v242, 0x3e0293ee, v242\n\t"
    "v_mul_f32_e32 v243, 0x3e0293ee, v243\n\t"
    "v_cvt_pk_bf16_f32 v242, v242, v243\n\t"
    "v_accvgpr_write_b32 a133, v242\n\t"
    "v_lshlrev_b32_e32 v242, 16, v6\n\t"
    "v_and_b32_e32 v243, 0xffff0000, v6\n\t"
    "v_mul_f32_e32 v242, 0x3e0293ee, v242\n\t"
    "v_mul_f32_e32 v243, 0x3e0293ee, v243\n\t"
    "v_cvt_pk_bf16_f32 v242, v242, v243\n\t"
    "v_accvgpr_write_b32 a134, v242\n\t"
    "v_lshlrev_b32_e32 v242, 16, v7\n\t"
    "v_and_b32_e32 v243, 0xffff0000, v7\n\t"
    "v_mul_f32_e32 v242, 0x3e0293ee, v242\n\t"
    "v_mul_f32_e32 v243, 0x3e0293ee, v243\n\t"
    "v_cvt_pk_bf16_f32 v242, v242, v243\n\t"
    "v_accvgpr_write_b32 a135, v242\n\t"
    "v_lshlrev_b32_e32 v242, 16, v8\n\t"
    "v_and_b32_e32 v243, 0xffff0000, v8\n\t"
    "v_mul_f32_e32 v242, 0x3e0293ee, v242\n\t"
    "v_mul_f32_e32 v243, 0x3e0293ee, v243\n\t"
    "v_cvt_pk_bf16_f32 v242, v242, v243\n\t"
    "v_accvgpr_write_b32 a136, v242\n\t"
    "v_lshlrev_b32_e32 v242, 16, v9\n\t"
    "v_and_b32_e32 v243, 0xffff0000, v9\n\t"
    "v_mul_f32_e32 v242, 0x3e0293ee, v242\n\t"
    "v_mul_f32_e32 v243, 0x3e0293ee, v243\n\t"
    "v_cvt_pk_bf16_f32 v242, v242, v243\n\t"
    "v_accvgpr_write_b32 a137, v242\n\t"
    "v_lshlrev_b32_e32 v242, 16, v10\n\t"
    "v_and_b32_e32 v243, 0xffff0000, v10\n\t"
    "v_mul_f32_e32 v242, 0x3e0293ee, v242\n\t"
    "v_mul_f32_e32 v243, 0x3e0293ee, v243\n\t"
    "v_cvt_pk_bf16_f32 v242, v242, v243\n\t"
    "v_accvgpr_write_b32 a138, v242\n\t"
    "v_lshlrev_b32_e32 v242, 16, v11\n\t"
    "v_and_b32_e32 v243, 0xffff0000, v11\n\t"
    "v_mul_f32_e32 v242, 0x3e0293ee, v242\n\t"
    "v_mul_f32_e32 v243, 0x3e0293ee, v243\n\t"
    "v_cvt_pk_bf16_f32 v242, v242, v243\n\t"
    "v_accvgpr_write_b32 a139, v242\n\t"
    "v_lshlrev_b32_e32 v242, 16, v12\n\t"
    "v_and_b32_e32 v243, 0xffff0000, v12\n\t"
    "v_mul_f32_e32 v242, 0x3e0293ee, v242\n\t"
    "v_mul_f32_e32 v243, 0x3e0293ee, v243\n\t"
    "v_cvt_pk_bf16_f32 v242, v242, v243\n\t"
    "v_accvgpr_write_b32 a140, v242\n\t"
    "v_lshlrev_b32_e32 v242, 16, v13\n\t"
    "v_and_b32_e32 v243, 0xffff0000, v13\n\t"
    "v_mul_f32_e32 v242, 0x3e0293ee, v242\n\t"
    "v_mul_f32_e32 v243, 0x3e0293ee, v243\n\t"
    "v_cvt_pk_bf16_f32 v242, v242, v243\n\t"
    "v_accvgpr_write_b32 a141, v242\n\t"
    "v_lshlrev_b32_e32 v242, 16, v14\n\t"
    "v_and_b32_e32 v243, 0xffff0000, v14\n\t"
    "v_mul_f32_e32 v242, 0x3e0293ee, v242\n\t"
    "v_mul_f32_e32 v243, 0x3e0293ee, v243\n\t"
    "v_cvt_pk_bf16_f32 v242, v242, v243\n\t"
    "v_accvgpr_write_b32 a142, v242\n\t"
    "v_lshlrev_b32_e32 v242, 16, v15\n\t"
    "v_and_b32_e32 v243, 0xffff0000, v15\n\t"
    "v_mul_f32_e32 v242, 0x3e0293ee, v242\n\t"
    "v_mul_f32_e32 v243, 0x3e0293ee, v243\n\t"
    "v_cvt_pk_bf16_f32 v242, v242, v243\n\t"
    "v_accvgpr_write_b32 a143, v242\n\t"
    "v_lshlrev_b32_e32 v242, 16, v16\n\t"
    "v_and_b32_e32 v243, 0xffff0000, v16\n\t"
    "v_mul_f32_e32 v242, 0x3e0293ee, v242\n\t"
    "v_mul_f32_e32 v243, 0x3e0293ee, v243\n\t"
    "v_cvt_pk_bf16_f32 v242, v242, v243\n\t"
    "v_accvgpr_write_b32 a144, v242\n\t"
    "v_lshlrev_b32_e32 v242, 16, v17\n\t"
    "v_and_b32_e32 v243, 0xffff0000, v17\n\t"
    "v_mul_f32_e32 v242, 0x3e0293ee, v242\n\t"
    "v_mul_f32_e32 v243, 0x3e0293ee, v243\n\t"
    "v_cvt_pk_bf16_f32 v242, v242, v243\n\t"
    "v_accvgpr_write_b32 a145, v242\n\t"
    "v_lshlrev_b32_e32 v242, 16, v18\n\t"
    "v_and_b32_e32 v243, 0xffff0000, v18\n\t"
    "v_mul_f32_e32 v242, 0x3e0293ee, v242\n\t"
    "v_mul_f32_e32 v243, 0x3e0293ee, v243\n\t"
    "v_cvt_pk_bf16_f32 v242, v242, v243\n\t"
    "v_accvgpr_write_b32 a146, v242\n\t"
    "v_lshlrev_b32_e32 v242, 16, v19\n\t"
    "v_and_b32_e32 v243, 0xffff0000, v19\n\t"
    "v_mul_f32_e32 v242, 0x3e0293ee, v242\n\t"
    "v_mul_f32_e32 v243, 0x3e0293ee, v243\n\t"
    "v_cvt_pk_bf16_f32 v242, v242, v243\n\t"
    "v_accvgpr_write_b32 a147, v242\n\t"
    "v_lshlrev_b32_e32 v242, 16, v20\n\t"
    "v_and_b32_e32 v243, 0xffff0000, v20\n\t"
    "v_mul_f32_e32 v242, 0x3e0293ee, v242\n\t"
    "v_mul_f32_e32 v243, 0x3e0293ee, v243\n\t"
    "v_cvt_pk_bf16_f32 v242, v242, v243\n\t"
    "v_accvgpr_write_b32 a148, v242\n\t"
    "v_lshlrev_b32_e32 v242, 16, v21\n\t"
    "v_and_b32_e32 v243, 0xffff0000, v21\n\t"
    "v_mul_f32_e32 v242, 0x3e0293ee, v242\n\t"
    "v_mul_f32_e32 v243, 0x3e0293ee, v243\n\t"
    "v_cvt_pk_bf16_f32 v242, v242, v243\n\t"
    "v_accvgpr_write_b32 a149, v242\n\t"
    "v_lshlrev_b32_e32 v242, 16, v22\n\t"
    "v_and_b32_e32 v243, 0xffff0000, v22\n\t"
    "v_mul_f32_e32 v242, 0x3e0293ee, v242\n\t"
    "v_mul_f32_e32 v243, 0x3e0293ee, v243\n\t"
    "v_cvt_pk_bf16_f32 v242, v242, v243\n\t"
    "v_accvgpr_write_b32 a150, v242\n\t"
    "v_lshlrev_b32_e32 v242, 16, v23\n\t"
    "v_and_b32_e32 v243, 0xffff0000, v23\n\t"
    "v_mul_f32_e32 v242, 0x3e0293ee, v242\n\t"
    "v_mul_f32_e32 v243, 0x3e0293ee, v243\n\t"
    "v_cvt_pk_bf16_f32 v242, v242, v243\n\t"
    "v_accvgpr_write_b32 a151, v242\n\t"
    "v_lshlrev_b32_e32 v242, 16, v24\n\t"
    "v_and_b32_e32 v243, 0xffff0000, v24\n\t"
    "v_mul_f32_e32 v242, 0x3e0293ee, v242\n\t"
    "v_mul_f32_e32 v243, 0x3e0293ee, v243\n\t"
    "v_cvt_pk_bf16_f32 v242, v242, v243\n\t"
    "v_accvgpr_write_b32 a152, v242\n\t"
    "v_lshlrev_b32_e32 v242, 16, v25\n\t"
    "v_and_b32_e32 v243, 0xffff0000, v25\n\t"
    "v_mul_f32_e32 v242, 0x3e0293ee, v242\n\t"
    "v_mul_f32_e32 v243, 0x3e0293ee, v243\n\t"
    "v_cvt_pk_bf16_f32 v242, v242, v243\n\t"
    "v_accvgpr_write_b32 a153, v242\n\t"
    "v_lshlrev_b32_e32 v242, 16, v26\n\t"
    "v_and_b32_e32 v243, 0xffff0000, v26\n\t"
    "v_mul_f32_e32 v242, 0x3e0293ee, v242\n\t"
    "v_mul_f32_e32 v243, 0x3e0293ee, v243\n\t"
    "v_cvt_pk_bf16_f32 v242, v242, v243\n\t"
    "v_accvgpr_write_b32 a154, v242\n\t"
    "v_lshlrev_b32_e32 v242, 16, v27\n\t"
    "v_and_b32_e32 v243, 0xffff0000, v27\n\t"
    "v_mul_f32_e32 v242, 0x3e0293ee, v242\n\t"
    "v_mul_f32_e32 v243, 0x3e0293ee, v243\n\t"
    "v_cvt_pk_bf16_f32 v242, v242, v243\n\t"
    "v_accvgpr_write_b32 a155, v242\n\t"
    "v_lshlrev_b32_e32 v242, 16, v28\n\t"
    "v_and_b32_e32 v243, 0xffff0000, v28\n\t"
    "v_mul_f32_e32 v242, 0x3e0293ee, v242\n\t"
    "v_mul_f32_e32 v243, 0x3e0293ee, v243\n\t"
    "v_cvt_pk_bf16_f32 v242, v242, v243\n\t"
    "v_accvgpr_write_b32 a156, v242\n\t"
    "v_lshlrev_b32_e32 v242, 16, v29\n\t"
    "v_and_b32_e32 v243, 0xffff0000, v29\n\t"
    "v_mul_f32_e32 v242, 0x3e0293ee, v242\n\t"
    "v_mul_f32_e32 v243, 0x3e0293ee, v243\n\t"
    "v_cvt_pk_bf16_f32 v242, v242, v243\n\t"
    "v_accvgpr_write_b32 a157, v242\n\t"
    "v_lshlrev_b32_e32 v242, 16, v30\n\t"
    "v_and_b32_e32 v243, 0xffff0000, v30\n\t"
    "v_mul_f32_e32 v242, 0x3e0293ee, v242\n\t"
    "v_mul_f32_e32 v243, 0x3e0293ee, v243\n\t"
    "v_cvt_pk_bf16_f32 v242, v242, v243\n\t"
    "v_accvgpr_write_b32 a158, v242\n\t"
    "v_lshlrev_b32_e32 v242, 16, v31\n\t"
    "v_and_b32_e32 v243, 0xffff0000, v31\n\t"
    "v_mul_f32_e32 v242, 0x3e0293ee, v242\n\t"
    "v_mul_f32_e32 v243, 0x3e0293ee, v243\n\t"
    "v_cvt_pk_bf16_f32 v242, v242, v243\n\t"
    "v_accvgpr_write_b32 a159, v242\n\t"
    "v_lshlrev_b32_e32 v242, 16, v32\n\t"
    "v_and_b32_e32 v243, 0xffff0000, v32\n\t"
    "v_mul_f32_e32 v242, 0x3e0293ee, v242\n\t"
    "v_mul_f32_e32 v243, 0x3e0293ee, v243\n\t"
    "v_cvt_pk_bf16_f32 v242, v242, v243\n\t"
    "v_accvgpr_write_b32 a160, v242\n\t"
    "v_lshlrev_b32_e32 v242, 16, v33\n\t"
    "v_and_b32_e32 v243, 0xffff0000, v33\n\t"
    "v_mul_f32_e32 v242, 0x3e0293ee, v242\n\t"
    "v_mul_f32_e32 v243, 0x3e0293ee, v243\n\t"
    "v_cvt_pk_bf16_f32 v242, v242, v243\n\t"
    "v_accvgpr_write_b32 a161, v242\n\t"
    "v_lshlrev_b32_e32 v242, 16, v34\n\t"
    "v_and_b32_e32 v243, 0xffff0000, v34\n\t"
    "v_mul_f32_e32 v242, 0x3e0293ee, v242\n\t"
    "v_mul_f32_e32 v243, 0x3e0293ee, v243\n\t"
    "v_cvt_pk_bf16_f32 v242, v242, v243\n\t"
    "v_accvgpr_write_b32 a162, v242\n\t"
    "v_lshlrev_b32_e32 v242, 16, v35\n\t"
    "v_and_b32_e32 v243, 0xffff0000, v35\n\t"
    "v_mul_f32_e32 v242, 0x3e0293ee, v242\n\t"
    "v_mul_f32_e32 v243, 0x3e0293ee, v243\n\t"
    "v_cvt_pk_bf16_f32 v242, v242, v243\n\t"
    "v_accvgpr_write_b32 a163, v242\n\t"
    "v_lshlrev_b32_e32 v242, 16, v36\n\t"
    "v_and_b32_e32 v243, 0xffff0000, v36\n\t"
    "v_mul_f32_e32 v242, 0x3e0293ee, v242\n\t"
    "v_mul_f32_e32 v243, 0x3e0293ee, v243\n\t"
    "v_cvt_pk_bf16_f32 v242, v242, v243\n\t"
    "v_accvgpr_write_b32 a164, v242\n\t"
    "v_lshlrev_b32_e32 v242, 16, v37\n\t"
    "v_and_b32_e32 v243, 0xffff0000, v37\n\t"
    "v_mul_f32_e32 v242, 0x3e0293ee, v242\n\t"
    "v_mul_f32_e32 v243, 0x3e0293ee, v243\n\t"
    "v_cvt_pk_bf16_f32 v242, v242, v243\n\t"
    "v_accvgpr_write_b32 a165, v242\n\t"
    "v_lshlrev_b32_e32 v242, 16, v38\n\t"
    "v_and_b32_e32 v243, 0xffff0000, v38\n\t"
    "v_mul_f32_e32 v242, 0x3e0293ee, v242\n\t"
    "v_mul_f32_e32 v243, 0x3e0293ee, v243\n\t"
    "v_cvt_pk_bf16_f32 v242, v242, v243\n\t"
    "v_accvgpr_write_b32 a166, v242\n\t"
    "v_lshlrev_b32_e32 v242, 16, v39\n\t"
    "v_and_b32_e32 v243, 0xffff0000, v39\n\t"
    "v_mul_f32_e32 v242, 0x3e0293ee, v242\n\t"
    "v_mul_f32_e32 v243, 0x3e0293ee, v243\n\t"
    "v_cvt_pk_bf16_f32 v242, v242, v243\n\t"
    "v_accvgpr_write_b32 a167, v242\n\t"
    "v_lshlrev_b32_e32 v242, 16, v40\n\t"
    "v_and_b32_e32 v243, 0xffff0000, v40\n\t"
    "v_mul_f32_e32 v242, 0x3e0293ee, v242\n\t"
    "v_mul_f32_e32 v243, 0x3e0293ee, v243\n\t"
    "v_cvt_pk_bf16_f32 v242, v242, v243\n\t"
    "v_accvgpr_write_b32 a168, v242\n\t"
    "v_lshlrev_b32_e32 v242, 16, v41\n\t"
    "v_and_b32_e32 v243, 0xffff0000, v41\n\t"
    "v_mul_f32_e32 v242, 0x3e0293ee, v242\n\t"
    "v_mul_f32_e32 v243, 0x3e0293ee, v243\n\t"
    "v_cvt_pk_bf16_f32 v242, v242, v243\n\t"
    "v_accvgpr_write_b32 a169, v242\n\t"
    "v_lshlrev_b32_e32 v242, 16, v42\n\t"
    "v_and_b32_e32 v243, 0xffff0000, v42\n\t"
    "v_mul_f32_e32 v242, 0x3e0293ee, v242\n\t"
    "v_mul_f32_e32 v243, 0x3e0293ee, v243\n\t"
    "v_cvt_pk_bf16_f32 v242, v242, v243\n\t"
    "v_accvgpr_write_b32 a170, v242\n\t"
    "v_lshlrev_b32_e32 v242, 16, v43\n\t"
    "v_and_b32_e32 v243, 0xffff0000, v43\n\t"
    "v_mul_f32_e32 v242, 0x3e0293ee, v242\n\t"
    "v_mul_f32_e32 v243, 0x3e0293ee, v243\n\t"
    "v_cvt_pk_bf16_f32 v242, v242, v243\n\t"
    "v_accvgpr_write_b32 a171, v242\n\t"
    "v_lshlrev_b32_e32 v242, 16, v44\n\t"
    "v_and_b32_e32 v243, 0xffff0000, v44\n\t"
    "v_mul_f32_e32 v242, 0x3e0293ee, v242\n\t"
    "v_mul_f32_e32 v243, 0x3e0293ee, v243\n\t"
    "v_cvt_pk_bf16_f32 v242, v242, v243\n\t"
    "v_accvgpr_write_b32 a172, v242\n\t"
    "v_lshlrev_b32_e32 v242, 16, v45\n\t"
    "v_and_b32_e32 v243, 0xffff0000, v45\n\t"
    "v_mul_f32_e32 v242, 0x3e0293ee, v242\n\t"
    "v_mul_f32_e32 v243, 0x3e0293ee, v243\n\t"
    "v_cvt_pk_bf16_f32 v242, v242, v243\n\t"
    "v_accvgpr_write_b32 a173, v242\n\t"
    "v_lshlrev_b32_e32 v242, 16, v46\n\t"
    "v_and_b32_e32 v243, 0xffff0000, v46\n\t"
    "v_mul_f32_e32 v242, 0x3e0293ee, v242\n\t"
    "v_mul_f32_e32 v243, 0x3e0293ee, v243\n\t"
    "v_cvt_pk_bf16_f32 v242, v242, v243\n\t"
    "v_accvgpr_write_b32 a174, v242\n\t"
    "v_lshlrev_b32_e32 v242, 16, v47\n\t"
    "v_and_b32_e32 v243, 0xffff0000, v47\n\t"
    "v_mul_f32_e32 v242, 0x3e0293ee, v242\n\t"
    "v_mul_f32_e32 v243, 0x3e0293ee, v243\n\t"
    "v_cvt_pk_bf16_f32 v242, v242, v243\n\t"
    "v_accvgpr_write_b32 a175, v242\n\t"
    "v_lshlrev_b32_e32 v242, 16, v48\n\t"
    "v_and_b32_e32 v243, 0xffff0000, v48\n\t"
    "v_mul_f32_e32 v242, 0x3e0293ee, v242\n\t"
    "v_mul_f32_e32 v243, 0x3e0293ee, v243\n\t"
    "v_cvt_pk_bf16_f32 v242, v242, v243\n\t"
    "v_accvgpr_write_b32 a176, v242\n\t"
    "v_lshlrev_b32_e32 v242, 16, v49\n\t"
    "v_and_b32_e32 v243, 0xffff0000, v49\n\t"
    "v_mul_f32_e32 v242, 0x3e0293ee, v242\n\t"
    "v_mul_f32_e32 v243, 0x3e0293ee, v243\n\t"
    "v_cvt_pk_bf16_f32 v242, v242, v243\n\t"
    "v_accvgpr_write_b32 a177, v242\n\t"
    "v_lshlrev_b32_e32 v242, 16, v50\n\t"
    "v_and_b32_e32 v243, 0xffff0000, v50\n\t"
    "v_mul_f32_e32 v242, 0x3e0293ee, v242\n\t"
    "v_mul_f32_e32 v243, 0x3e0293ee, v243\n\t"
    "v_cvt_pk_bf16_f32 v242, v242, v243\n\t"
    "v_accvgpr_write_b32 a178, v242\n\t"
    "v_lshlrev_b32_e32 v242, 16, v51\n\t"
    "v_and_b32_e32 v243, 0xffff0000, v51\n\t"
    "v_mul_f32_e32 v242, 0x3e0293ee, v242\n\t"
    "v_mul_f32_e32 v243, 0x3e0293ee, v243\n\t"
    "v_cvt_pk_bf16_f32 v242, v242, v243\n\t"
    "v_accvgpr_write_b32 a179, v242\n\t"
    "v_lshlrev_b32_e32 v242, 16, v52\n\t"
    "v_and_b32_e32 v243, 0xffff0000, v52\n\t"
    "v_mul_f32_e32 v242, 0x3e0293ee, v242\n\t"
    "v_mul_f32_e32 v243, 0x3e0293ee, v243\n\t"
    "v_cvt_pk_bf16_f32 v242, v242, v243\n\t"
    "v_accvgpr_write_b32 a180, v242\n\t"
    "v_lshlrev_b32_e32 v242, 16, v53\n\t"
    "v_and_b32_e32 v243, 0xffff0000, v53\n\t"
    "v_mul_f32_e32 v242, 0x3e0293ee, v242\n\t"
    "v_mul_f32_e32 v243, 0x3e0293ee, v243\n\t"
    "v_cvt_pk_bf16_f32 v242, v242, v243\n\t"
    "v_accvgpr_write_b32 a181, v242\n\t"
    "v_lshlrev_b32_e32 v242, 16, v54\n\t"
    "v_and_b32_e32 v243, 0xffff0000, v54\n\t"
    "v_mul_f32_e32 v242, 0x3e0293ee, v242\n\t"
    "v_mul_f32_e32 v243, 0x3e0293ee, v243\n\t"
    "v_cvt_pk_bf16_f32 v242, v242, v243\n\t"
    "v_accvgpr_write_b32 a182, v242\n\t"
    "v_lshlrev_b32_e32 v242, 16, v55\n\t"
    "v_and_b32_e32 v243, 0xffff0000, v55\n\t"
    "v_mul_f32_e32 v242, 0x3e0293ee, v242\n\t"
    "v_mul_f32_e32 v243, 0x3e0293ee, v243\n\t"
    "v_cvt_pk_bf16_f32 v242, v242, v243\n\t"
    "v_accvgpr_write_b32 a183, v242\n\t"
    "v_lshlrev_b32_e32 v242, 16, v56\n\t"
    "v_and_b32_e32 v243, 0xffff0000, v56\n\t"
    "v_mul_f32_e32 v242, 0x3e0293ee, v242\n\t"
    "v_mul_f32_e32 v243, 0x3e0293ee, v243\n\t"
    "v_cvt_pk_bf16_f32 v242, v242, v243\n\t"
    "v_accvgpr_write_b32 a184, v242\n\t"
    "v_lshlrev_b32_e32 v242, 16, v57\n\t"
    "v_and_b32_e32 v243, 0xffff0000, v57\n\t"
    "v_mul_f32_e32 v242, 0x3e0293ee, v242\n\t"
    "v_mul_f32_e32 v243, 0x3e0293ee, v243\n\t"
    "v_cvt_pk_bf16_f32 v242, v242, v243\n\t"
    "v_accvgpr_write_b32 a185, v242\n\t"
    "v_lshlrev_b32_e32 v242, 16, v58\n\t"
    "v_and_b32_e32 v243, 0xffff0000, v58\n\t"
    "v_mul_f32_e32 v242, 0x3e0293ee, v242\n\t"
    "v_mul_f32_e32 v243, 0x3e0293ee, v243\n\t"
    "v_cvt_pk_bf16_f32 v242, v242, v243\n\t"
    "v_accvgpr_write_b32 a186, v242\n\t"
    "v_lshlrev_b32_e32 v242, 16, v59\n\t"
    "v_and_b32_e32 v243, 0xffff0000, v59\n\t"
    "v_mul_f32_e32 v242, 0x3e0293ee, v242\n\t"
    "v_mul_f32_e32 v243, 0x3e0293ee, v243\n\t"
    "v_cvt_pk_bf16_f32 v242, v242, v243\n\t"
    "v_accvgpr_write_b32 a187, v242\n\t"
    "v_lshlrev_b32_e32 v242, 16, v60\n\t"
    "v_and_b32_e32 v243, 0xffff0000, v60\n\t"
    "v_mul_f32_e32 v242, 0x3e0293ee, v242\n\t"
    "v_mul_f32_e32 v243, 0x3e0293ee, v243\n\t"
    "v_cvt_pk_bf16_f32 v242, v242, v243\n\t"
    "v_accvgpr_write_b32 a188, v242\n\t"
    "v_lshlrev_b32_e32 v242, 16, v61\n\t"
    "v_and_b32_e32 v243, 0xffff0000, v61\n\t"
    "v_mul_f32_e32 v242, 0x3e0293ee, v242\n\t"
    "v_mul_f32_e32 v243, 0x3e0293ee, v243\n\t"
    "v_cvt_pk_bf16_f32 v242, v242, v243\n\t"
    "v_accvgpr_write_b32 a189, v242\n\t"
    "v_lshlrev_b32_e32 v242, 16, v62\n\t"
    "v_and_b32_e32 v243, 0xffff0000, v62\n\t"
    "v_mul_f32_e32 v242, 0x3e0293ee, v242\n\t"
    "v_mul_f32_e32 v243, 0x3e0293ee, v243\n\t"
    "v_cvt_pk_bf16_f32 v242, v242, v243\n\t"
    "v_accvgpr_write_b32 a190, v242\n\t"
    "v_lshlrev_b32_e32 v242, 16, v63\n\t"
    "v_and_b32_e32 v243, 0xffff0000, v63\n\t"
    "v_mul_f32_e32 v242, 0x3e0293ee, v242\n\t"
    "v_mul_f32_e32 v243, 0x3e0293ee, v243\n\t"
    "v_cvt_pk_bf16_f32 v242, v242, v243\n\t"
    "v_accvgpr_write_b32 a191, v242\n\t"
    "s_waitcnt vmcnt(0)\n\t"
    "s_barrier\n\t"
    "ds_read_b128 a[192:195], v224 offset:0\n\t"
    "ds_read_b128 a[196:199], v225 offset:0\n\t"
    "ds_read_b128 a[200:203], v226 offset:0\n\t"
    "ds_read_b128 a[204:207], v227 offset:0\n\t"
    "ds_read_b128 a[208:211], v224 offset:128\n\t"
    "ds_read_b128 a[212:215], v225 offset:128\n\t"
    "ds_read_b128 a[216:219], v226 offset:128\n\t"
    "ds_read_b128 a[220:223], v227 offset:128\n\t"
    "ds_read_b128 a[224:227], v224 offset:8192\n\t"
    "ds_read_b128 a[228:231], v225 offset:8192\n\t"
    "ds_read_b128 a[232:235], v226 offset:8192\n\t"
    "ds_read_b128 a[236:239], v227 offset:8192\n\t"
    "ds_read_b128 a[240:243], v224 offset:8320\n\t"
    "ds_read_b128 a[244:247], v225 offset:8320\n\t"
    "ds_read_b128 a[248:251], v226 offset:8320\n\t"
    "ds_read_b128 a[252:255], v227 offset:8320\n\t"
    "s_waitcnt lgkmcnt(0)\n\t"
    "v_mfma_f32_32x32x16_bf16 v[0:15], a[192:195], a[128:131], 0\n\t"
    "v_mfma_f32_32x32x16_bf16 v[16:31], a[192:195], a[160:163], 0\n\t"
    "v_mfma_f32_32x32x16_bf16 v[0:15], a[196:199], a[132:135], v[0:15]\n\t"
    "v_mfma_f32_32x32x16_bf16 v[16:31], a[196:199], a[164:167], v[16:31]\n\t"
    "v_mfma_f32_32x32x16_bf16 v[0:15], a[200:203], a[136:139], v[0:15]\n\t"
    "v_mfma_f32_32x32x16_bf16 v[16:31], a[200:203], a[168:171], v[16:31]\n\t"
    "v_mfma_f32_32x32x16_bf16 v[0:15], a[204:207], a[140:143], v[0:15]\n\t"
    "v_mfma_f32_32x32x16_bf16 v[16:31], a[204:207], a[172:175], v[16:31]\n\t"
    "v_mfma_f32_32x32x16_bf16 v[0:15], a[208:211], a[144:147], v[0:15]\n\t"
    "v_mfma_f32_32x32x16_bf16 v[16:31], a[208:211], a[176:179], v[16:31]\n\t"
    "v_mfma_f32_32x32x16_bf16 v[0:15], a[212:215], a[148:151], v[0:15]\n\t"
    "v_mfma_f32_32x32x16_bf16 v[16:31], a[212:215], a[180:183], v[16:31]\n\t"
    "v_mfma_f32_32x32x16_bf16 v[0:15], a[216:219], a[152:155], v[0:15]\n\t"
    "v_mfma_f32_32x32x16_bf16 v[16:31], a[216:219], a[184:187], v[16:31]\n\t"
    "v_mfma_f32_32x32x16_bf16 v[0:15], a[220:223], a[156:159], v[0:15]\n\t"
    "v_mfma_f32_32x32x16_bf16 v[16:31], a[220:223], a[188:191], v[16:31]\n\t"
    "v_mfma_f32_32x32x16_bf16 v[32:47], a[224:227], a[128:131], 0\n\t"
    "v_mfma_f32_32x32x16_bf16 v[48:63], a[224:227], a[160:163], 0\n\t"
    "v_mfma_f32_32x32x16_bf16 v[32:47], a[228:231], a[132:135], v[32:47]\n\t"
    "v_mfma_f32_32x32x16_bf16 v[48:63], a[228:231], a[164:167], v[48:63]\n\t"
    "v_mfma_f32_32x32x16_bf16 v[32:47], a[232:235], a[136:139], v[32:47]\n\t"
    "v_mfma_f32_32x32x16_bf16 v[48:63], a[232:235], a[168:171], v[48:63]\n\t"
    "v_mfma_f32_32x32x16_bf16 v[32:47], a[236:239], a[140:143], v[32:47]\n\t"
    "v_mfma_f32_32x32x16_bf16 v[48:63], a[236:239], a[172:175], v[48:63]\n\t"
    "v_mfma_f32_32x32x16_bf16 v[32:47], a[240:243], a[144:147], v[32:47]\n\t"
    "v_mfma_f32_32x32x16_bf16 v[48:63], a[240:243], a[176:179], v[48:63]\n\t"
    "v_mfma_f32_32x32x16_bf16 v[32:47], a[244:247], a[148:151], v[32:47]\n\t"
    "v_mfma_f32_32x32x16_bf16 v[48:63], a[244:247], a[180:183], v[48:63]\n\t"
    "v_mfma_f32_32x32x16_bf16 v[32:47], a[248:251], a[152:155], v[32:47]\n\t"
    "v_mfma_f32_32x32x16_bf16 v[48:63], a[248:251], a[184:187], v[48:63]\n\t"
    "v_mfma_f32_32x32x16_bf16 v[32:47], a[252:255], a[156:159], v[32:47]\n\t"
    "v_mfma_f32_32x32x16_bf16 v[48:63], a[252:255], a[188:191], v[48:63]\n\t"
    "s_nop 7\n\t"
    "s_nop 7\n\t"
    "v_max3_f32 v242, v0, v1, v2\n\t"
    "v_max3_f32 v242, v242, v3, v4\n\t"
    "v_max3_f32 v242, v242, v5, v6\n\t"
    "v_max3_f32 v242, v242, v7, v8\n\t"
    "v_max3_f32 v242, v242, v9, v10\n\t"
    "v_max3_f32 v242, v242, v11, v12\n\t"
    "v_max3_f32 v242, v242, v13, v14\n\t"
    "v_max3_f32 v242, v242, v15, v32\n\t"
    "v_max3_f32 v242, v242, v33, v34\n\t"
    "v_max3_f32 v242, v242, v35, v36\n\t"
    "v_max3_f32 v242, v242, v37, v38\n\t"
    "v_max3_f32 v242, v242, v39, v40\n\t"
    "v_max3_f32 v242, v242, v41, v42\n\t"
    "v_max3_f32 v242, v242, v43, v44\n\t"
    "v_max3_f32 v242, v242, v45, v46\n\t"
    "v_max_f32_e32 v242, v242, v47\n\t"
    "v_mov_b32_e32 v243, v242\n\t"
    "s_nop 1\n\t"
    "v_permlane32_swap_b32_e32 v242, v243\n\t"
    "v_max_f32_e32 v240, v242, v243\n\t"
    "v_sub_f32_e32 v0, v0, v240\n\t"
    "v_sub_f32_e32 v1, v1, v240\n\t"
    "v_sub_f32_e32 v2, v2, v240\n\t"
    "v_sub_f32_e32 v3, v3, v240\n\t"
    "v_sub_f32_e32 v4, v4, v240\n\t"
    "v_sub_f32_e32 v5, v5, v240\n\t"
    "v_sub_f32_e32 v6, v6, v240\n\t"
    "v_sub_f32_e32 v7, v7, v240\n\t"
    "v_sub_f32_e32 v8, v8, v240\n\t"
    "v_sub_f32_e32 v9, v9, v240\n\t"
    "v_sub_f32_e32 v10, v10, v240\n\t"
    "v_sub_f32_e32 v11, v11, v240\n\t"
    "v_sub_f32_e32 v12, v12, v240\n\t"
    "v_sub_f32_e32 v13, v13, v240\n\t"
    "v_sub_f32_e32 v14, v14, v240\n\t"
    "v_sub_f32_e32 v15, v15, v240\n\t"
    "v_sub_f32_e32 v32, v32, v240\n\t"
    "v_sub_f32_e32 v33, v33, v240\n\t"
    "v_sub_f32_e32 v34, v34, v240\n\t"
    "v_sub_f32_e32 v35, v35, v240\n\t"
    "v_sub_f32_e32 v36, v36, v240\n\t"
    "v_sub_f32_e32 v37, v37, v240\n\t"
    "v_sub_f32_e32 v38, v38, v240\n\t"
    "v_sub_f32_e32 v39, v39, v240\n\t"
    "v_sub_f32_e32 v40, v40, v240\n\t"
    "v_sub_f32_e32 v41, v41, v240\n\t"
    "v_sub_f32_e32 v42, v42, v240\n\t"
    "v_sub_f32_e32 v43, v43, v240\n\t"
    "v_sub_f32_e32 v44, v44, v240\n\t"
    "v_sub_f32_e32 v45, v45, v240\n\t"
    "v_sub_f32_e32 v46, v46, v240\n\t"
    "v_sub_f32_e32 v47, v47, v240\n\t"
    "v_sub_f32_e32 v243, 0, v240\n\t"
    "v_mov_b32_e32 v128, v243\n\t"
    "v_mov_b32_e32 v129, v243\n\t"
    "v_mov_b32_e32 v130, v243\n\t"
    "v_mov_b32_e32 v131, v243\n\t"
    "v_mov_b32_e32 v132, v243\n\t"
    "v_mov_b32_e32 v133, v243\n\t"
    "v_mov_b32_e32 v134, v243\n\t"
    "v_mov_b32_e32 v135, v243\n\t"
    "v_mov_b32_e32 v136, v243\n\t"
    "v_mov_b32_e32 v137, v243\n\t"
    "v_mov_b32_e32 v138, v243\n\t"
    "v_mov_b32_e32 v139, v243\n\t"
    "v_mov_b32_e32 v140, v243\n\t"
    "v_mov_b32_e32 v141, v243\n\t"
    "v_mov_b32_e32 v142, v243\n\t"
    "v_mov_b32_e32 v143, v243\n\t"
    "v_max3_f32 v242, v16, v17, v18\n\t"
    "v_max3_f32 v242, v242, v19, v20\n\t"
    "v_max3_f32 v242, v242, v21, v22\n\t"
    "v_max3_f32 v242, v242, v23, v24\n\t"
    "v_max3_f32 v242, v242, v25, v26\n\t"
    "v_max3_f32 v242, v242, v27, v28\n\t"
    "v_max3_f32 v242, v242, v29, v30\n\t"
    "v_max3_f32 v242, v242, v31, v48\n\t"
    "v_max3_f32 v242, v242, v49, v50\n\t"
    "v_max3_f32 v242, v242, v51, v52\n\t"
    "v_max3_f32 v242, v242, v53, v54\n\t"
    "v_max3_f32 v242, v242, v55, v56\n\t"
    "v_max3_f32 v242, v242, v57, v58\n\t"
    "v_max3_f32 v242, v242, v59, v60\n\t"
    "v_max3_f32 v242, v242, v61, v62\n\t"
    "v_max_f32_e32 v242, v242, v63\n\t"
    "v_mov_b32_e32 v243, v242\n\t"
    "s_nop 1\n\t"
    "v_permlane32_swap_b32_e32 v242, v243\n\t"
    "v_max_f32_e32 v241, v242, v243\n\t"
    "v_sub_f32_e32 v16, v16, v241\n\t"
    "v_sub_f32_e32 v17, v17, v241\n\t"
    "v_sub_f32_e32 v18, v18, v241\n\t"
    "v_sub_f32_e32 v19, v19, v241\n\t"
    "v_sub_f32_e32 v20, v20, v241\n\t"
    "v_sub_f32_e32 v21, v21, v241\n\t"
    "v_sub_f32_e32 v22, v22, v241\n\t"
    "v_sub_f32_e32 v23, v23, v241\n\t"
    "v_sub_f32_e32 v24, v24, v241\n\t"
    "v_sub_f32_e32 v25, v25, v241\n\t"
    "v_sub_f32_e32 v26, v26, v241\n\t"
    "v_sub_f32_e32 v27, v27, v241\n\t"
    "v_sub_f32_e32 v28, v28, v241\n\t"
    "v_sub_f32_e32 v29, v29, v241\n\t"
    "v_sub_f32_e32 v30, v30, v241\n\t"
    "v_sub_f32_e32 v31, v31, v241\n\t"
    "v_sub_f32_e32 v48, v48, v241\n\t"
    "v_sub_f32_e32 v49, v49, v241\n\t"
    "v_sub_f32_e32 v50, v50, v241\n\t"
    "v_sub_f32_e32 v51, v51, v241\n\t"
    "v_sub_f32_e32 v52, v52, v241\n\t"
    "v_sub_f32_e32 v53, v53, v241\n\t"
    "v_sub_f32_e32 v54, v54, v241\n\t"
    "v_sub_f32_e32 v55, v55, v241\n\t"
    "v_sub_f32_e32 v56, v56, v241\n\t"
    "v_sub_f32_e32 v57, v57, v241\n\t"
    "v_sub_f32_e32 v58, v58, v241\n\t"
    "v_sub_f32_e32 v59, v59, v241\n\t"
    "v_sub_f32_e32 v60, v60, v241\n\t"
    "v_sub_f32_e32 v61, v61, v241\n\t"
    "v_sub_f32_e32 v62, v62, v241\n\t"
    "v_sub_f32_e32 v63, v63, v241\n\t"
    "v_sub_f32_e32 v243, 0, v241\n\t"
    "v_mov_b32_e32 v144, v243\n\t"
    "v_mov_b32_e32 v145, v243\n\t"
    "v_mov_b32_e32 v146, v243\n\t"
    "v_mov_b32_e32 v147, v243\n\t"
    "v_mov_b32_e32 v148, v243\n\t"
    "v_mov_b32_e32 v149, v243\n\t"
    "v_mov_b32_e32 v150, v243\n\t"
    "v_mov_b32_e32 v151, v243\n\t"
    "v_mov_b32_e32 v152, v243\n\t"
    "v_mov_b32_e32 v153, v243\n\t"
    "v_mov_b32_e32 v154, v243\n\t"
    "v_mov_b32_e32 v155, v243\n\t"
    "v_mov_b32_e32 v156, v243\n\t"
    "v_mov_b32_e32 v157, v243\n\t"
    "v_mov_b32_e32 v158, v243\n\t"
    "v_mov_b32_e32 v159, v243\n\t"
    "s_barrier\n\t"
    "s_mov_b32 m0, s40\n\t"
    "buffer_load_dwordx4 v229, %[srdK], s50 offen offset:0 lds\n\t"
    "buffer_load_dwordx4 v230, %[srdK], s50 offen offset:1024 lds\n\t"
    "buffer_load_dwordx4 v229, %[srdK], s50 offen offset:2048 lds\n\t"
    "buffer_load_dwordx4 v230, %[srdK], s50 offen offset:3072 lds\n\t"
    "s_mov_b32 m0, s46\n\t"
    "buffer_load_dwordx4 v231, %[srdV], s51 offen lds\n\t"
    "s_mov_b32 m0, s47\n\t"
    "s_add_i32 s52, s51, 128\n\t"
    "buffer_load_dwordx4 v231, %[srdV], s52 offen lds\n\t"
    "s_mov_b32 m0, s48\n\t"
    "s_add_i32 s52, s51, 2048\n\t"
    "buffer_load_dwordx4 v231, %[srdV], s52 offen lds\n\t"
    "s_mov_b32 m0, s49\n\t"
    "s_add_i32 s52, s51, 2176\n\t"
    "buffer_load_dwordx4 v231, %[srdV], s52 offen lds\n\t"
    "s_add_i32 s50, s50, 16384\n\t"
    "s_add_i32 s51, s51, 16384\n\t"
    "ds_read_b128 a[192:195], v224 offset:16384\n\t"
    "ds_read_b128 a[196:199], v225 offset:16384\n\t"
    "ds_read_b128 a[200:203], v226 offset:16384\n\t"
    "ds_read_b128 a[204:207], v227 offset:16384\n\t"
    "ds_read_b128 a[208:211], v224 offset:16512\n\t"
    "ds_read_b128 a[212:215], v225 offset:16512\n\t"
    "ds_read_b128 a[216:219], v226 offset:16512\n\t"
    "ds_read_b128 a[220:223], v227 offset:16512\n\t"
    "ds_read_b128 a[224:227], v224 offset:24576\n\t"
    "ds_read_b128 a[228:231], v225 offset:24576\n\t"
    "ds_read_b128 a[232:235], v226 offset:24576\n\t"
    "ds_read_b128 a[236:239], v227 offset:24576\n\t"
    "ds_read_b128 a[240:243], v224 offset:24704\n\t"
    "ds_read_b128 a[244:247], v225 offset:24704\n\t"
    "ds_read_b128 a[248:251], v226 offset:24704\n\t"
    "ds_read_b128 a[252:255], v227 offset:24704\n\t"
    "v_exp_f32_e32 v0, v0\n\t"
    "s_nop 0\n\t"
    "v_exp_f32_e32 v1, v1\n\t"
    "s_nop 0\n\t"
    "v_exp_f32_e32 v2, v2\n\t"
    "s_nop 0\n\t"
    "v_add_f32_e32 v232, v0, v2\n\t"
    "v_exp_f32_e32 v3, v3\n\t"
    "s_nop 0\n\t"
    "v_add_f32_e32 v233, v1, v3\n\t"
    "v_exp_f32_e32 v4, v4\n\t"
    "s_nop 0\n\t"
    "v_add_f32_e32 v232, v232, v4\n\t"
    "v_exp_f32_e32 v5, v5\n\t"
    "s_nop 0\n\t"
    "v_add_f32_e32 v233, v233, v5\n\t"
    "v_exp_f32_e32 v6, v6\n\t"
    "s_nop 0\n\t"
    "v_add_f32_e32 v232, v232, v6\n\t"
    "v_exp_f32_e32 v7, v7\n\t"
    "s_nop 0\n\t"
    "v_add_f32_e32 v233, v233, v7\n\t"
    "v_exp_f32_e32 v16, v16\n\t"
    "s_nop 0\n\t"
    "v_exp_f32_e32 v17, v17\n\t"
    "s_nop 0\n\t"
    "v_exp_f32_e32 v18, v18\n\t"
    "s_nop 0\n\t"
    "v_add_f32_e32 v234, v16, v18\n\t"
    "v_exp_f32_e32 v19, v19\n\t"
    "s_nop 0\n\t"
    "v_add_f32_e32 v235, v17, v19\n\t"
    "v_exp_f32_e32 v20, v20\n\t"
    "s_nop 0\n\t"
    "v_add_f32_e32 v234, v234, v20\n\t"
    "v_exp_f32_e32 v21, v21\n\t"
    "s_nop 0\n\t"
    "v_add_f32_e32 v235, v235, v21\n\t"
    "v_exp_f32_e32 v22, v22\n\t"
    "s_nop 0\n\t"
    "v_add_f32_e32 v234, v234, v22\n\t"
    "v_exp_f32_e32 v23, v23\n\t"
    "s_nop 0\n\t"
    "v_add_f32_e32 v235, v235, v23\n\t"
    "v_exp_f32_e32 v8, v8\n\t"
    "s_nop 0\n\t"
    "v_add_f32_e32 v232, v232, v8\n\t"
    "v_exp_f32_e32 v9, v9\n\t"
    "s_nop 0\n\t"
    "v_add_f32_e32 v233, v233, v9\n\t"
    "v_exp_f32_e32 v10, v10\n\t"
    "s_nop 0\n\t"
    "v_add_f32_e32 v232, v232, v10\n\t"
    "v_exp_f32_e32 v11, v11\n\t"
    "s_nop 0\n\t"
    "v_add_f32_e32 v233, v233, v11\n\t"
    "v_exp_f32_e32 v12, v12\n\t"
    "s_nop 0\n\t"
    "v_add_f32_e32 v232, v232, v12\n\t"
    "v_exp_f32_e32 v13, v13\n\t"
    "s_nop 0\n\t"
    "v_add_f32_e32 v233, v233, v13\n\t"
    "v_exp_f32_e32 v14, v14\n\t"
    "s_nop 0\n\t"
    "v_add_f32_e32 v232, v232, v14\n\t"
    "v_exp_f32_e32 v15, v15\n\t"
    "s_nop 0\n\t"
    "v_add_f32_e32 v233, v233, v15\n\t"
    "v_exp_f32_e32 v24, v24\n\t"
    "s_nop 0\n\t"
    "v_add_f32_e32 v234, v234, v24\n\t"
    "v_exp_f32_e32 v25, v25\n\t"
    "s_nop 0\n\t"
    "v_add_f32_e32 v235, v235, v25\n\t"
    "v_exp_f32_e32 v26, v26\n\t"
    "s_nop 0\n\t"
    "v_add_f32_e32 v234, v234, v26\n\t"
    "v_exp_f32_e32 v27, v27\n\t"
    "s_nop 0\n\t"
    "v_add_f32_e32 v235, v235, v27\n\t"
    "v_exp_f32_e32 v28, v28\n\t"
    "s_nop 0\n\t"
    "v_add_f32_e32 v234, v234, v28\n\t"
    "v_exp_f32_e32 v29, v29\n\t"
    "s_nop 0\n\t"
    "v_add_f32_e32 v235, v235, v29\n\t"
    "v_exp_f32_e32 v30, v30\n\t"
    "s_nop 0\n\t"
    "v_add_f32_e32 v234, v234, v30\n\t"
    "v_exp_f32_e32 v31, v31\n\t"
    "s_nop 0\n\t"
    "v_add_f32_e32 v235, v235, v31\n\t"
    "s_waitcnt lgkmcnt(0)\n\t"
    "s_mov_b32 s53, 15\n\t"
    "LOOP_%=:\n\t"
    "v_mfma_f32_32x32x16_bf16 v[64:79], a[192:195], a[128:131], v[128:143]\n\t"
    "v_cvt_pk_bf16_f32 v0, v0, v1\n\t"
    "v_cvt_pk_bf16_f32 v1, v2, v3\n\t"
    "v_cvt_pk_bf16_f32 v2, v4, v5\n\t"
    "v_cvt_pk_bf16_f32 v3, v6, v7\n\t"
    "ds_read_b64_tr_b16 v[160:161], v228 offset:0\n\t"
    "ds_read_b64_tr_b16 v[162:163], v228 offset:2048\n\t"
    "v_mfma_f32_32x32x16_bf16 v[80:95], a[192:195], a[160:163], v[144:159]\n\t"
    "v_cvt_pk_bf16_f32 v16, v16, v17\n\t"
    "v_cvt_pk_bf16_f32 v17, v18, v19\n\t"
    "v_cvt_pk_bf16_f32 v18, v20, v21\n\t"
    "ds_read_b64_tr_b16 v[176:177], v228 offset:512\n\t"
    "v_mfma_f32_32x32x16_bf16 v[64:79], a[196:199], a[132:135], v[64:79]\n\t"
    "v_cvt_pk_bf16_f32 v19, v22, v23\n\t"
    "v_cvt_pk_bf16_f32 v8, v8, v9\n\t"
    "v_cvt_pk_bf16_f32 v9, v10, v11\n\t"
    "ds_read_b64_tr_b16 v[178:179], v228 offset:2560\n\t"
    "v_mfma_f32_32x32x16_bf16 v[80:95], a[196:199], a[164:167], v[80:95]\n\t"
    "v_cvt_pk_bf16_f32 v10, v12, v13\n\t"
    "v_cvt_pk_bf16_f32 v11, v14, v15\n\t"
    "v_cvt_pk_bf16_f32 v24, v24, v25\n\t"
    "ds_read_b64_tr_b16 v[192:193], v228 offset:1024\n\t"
    "v_mfma_f32_32x32x16_bf16 v[64:79], a[200:203], a[136:139], v[64:79]\n\t"
    "v_cvt_pk_bf16_f32 v25, v26, v27\n\t"
    "v_cvt_pk_bf16_f32 v26, v28, v29\n\t"
    "v_cvt_pk_bf16_f32 v27, v30, v31\n\t"
    "ds_read_b64_tr_b16 v[194:195], v228 offset:3072\n\t"
    "ds_read_b64_tr_b16 v[208:209], v228 offset:1536\n\t"
    "v_mfma_f32_32x32x16_bf16 v[80:95], a[200:203], a[168:171], v[80:95]\n\t"
    "v_exp_f32_e32 v32, v32\n\t"
    "v_exp_f32_e32 v33, v33\n\t"
    "v_add_f32_e32 v232, v232, v32\n\t"
    "ds_read_b64_tr_b16 v[210:211], v228 offset:3584\n\t"
    "v_mfma_f32_32x32x16_bf16 v[64:79], a[204:207], a[140:143], v[64:79]\n\t"
    "v_exp_f32_e32 v34, v34\n\t"
    "v_add_f32_e32 v233, v233, v33\n\t"
    "v_exp_f32_e32 v35, v35\n\t"
    "ds_read_b64_tr_b16 v[164:165], v228 offset:4096\n\t"
    "v_mfma_f32_32x32x16_bf16 v[80:95], a[204:207], a[172:175], v[80:95]\n\t"
    "v_add_f32_e32 v232, v232, v34\n\t"
    "v_exp_f32_e32 v36, v36\n\t"
    "v_add_f32_e32 v233, v233, v35\n\t"
    "ds_read_b64_tr_b16 v[166:167], v228 offset:6144\n\t"
    "v_mfma_f32_32x32x16_bf16 v[64:79], a[208:211], a[144:147], v[64:79]\n\t"
    "v_exp_f32_e32 v37, v37\n\t"
    "v_add_f32_e32 v232, v232, v36\n\t"
    "v_exp_f32_e32 v38, v38\n\t"
    "ds_read_b64_tr_b16 v[180:181], v228 offset:4608\n\t"
    "ds_read_b64_tr_b16 v[182:183], v228 offset:6656\n\t"
    "v_mfma_f32_32x32x16_bf16 v[80:95], a[208:211], a[176:179], v[80:95]\n\t"
    "v_add_f32_e32 v233, v233, v37\n\t"
    "v_exp_f32_e32 v39, v39\n\t"
    "v_cvt_pk_bf16_f32 v32, v32, v33\n\t"
    "ds_read_b64_tr_b16 v[196:197], v228 offset:5120\n\t"
    "v_mfma_f32_32x32x16_bf16 v[64:79], a[212:215], a[148:151], v[64:79]\n\t"
    "v_cvt_pk_bf16_f32 v33, v34, v35\n\t"
    "v_cvt_pk_bf16_f32 v34, v36, v37\n\t"
    "v_cvt_pk_bf16_f32 v35, v38, v39\n\t"
    "v_add_f32_e32 v232, v232, v38\n\t"
    "ds_read_b64_tr_b16 v[198:199], v228 offset:7168\n\t"
    "v_mfma_f32_32x32x16_bf16 v[80:95], a[212:215], a[180:183], v[80:95]\n\t"
    "v_exp_f32_e32 v48, v48\n\t"
    "v_add_f32_e32 v233, v233, v39\n\t"
    "v_exp_f32_e32 v49, v49\n\t"
    "ds_read_b64_tr_b16 v[212:213], v228 offset:5632\n\t"
    "v_mfma_f32_32x32x16_bf16 v[64:79], a[216:219], a[152:155], v[64:79]\n\t"
    "v_add_f32_e32 v234, v234, v48\n\t"
    "v_exp_f32_e32 v50, v50\n\t"
    "v_add_f32_e32 v235, v235, v49\n\t"
    "ds_read_b64_tr_b16 v[214:215], v228 offset:7680\n\t"
    "v_mfma_f32_32x32x16_bf16 v[80:95], a[216:219], a[184:187], v[80:95]\n\t"
    "v_exp_f32_e32 v51, v51\n\t"
    "v_add_f32_e32 v234, v234, v50\n\t"
    "v_exp_f32_e32 v52, v52\n\t"
    "ds_read_b64_tr_b16 v[168:169], v228 offset:8192\n\t"
    "ds_read_b64_tr_b16 v[170:171], v228 offset:10240\n\t"
    "v_mfma_f32_32x32x16_bf16 v[64:79], a[220:223], a[156:159], v[64:79]\n\t"
    "v_add_f32_e32 v235, v235, v51\n\t"
    "v_exp_f32_e32 v53, v53\n\t"
    "v_add_f32_e32 v234, v234, v52\n\t"
    "ds_read_b64_tr_b16 v[184:185], v228 offset:8704\n\t"
    "v_mfma_f32_32x32x16_bf16 v[80:95], a[220:223], a[188:191], v[80:95]\n\t"
    "v_exp_f32_e32 v54, v54\n\t"
    "v_add_f32_e32 v235, v235, v53\n\t"
    "v_exp_f32_e32 v55, v55\n\t"
    "ds_read_b64_tr_b16 v[186:187], v228 offset:10752\n\t"
    "v_mfma_f32_32x32x16_bf16 v[96:111], a[224:227], a[128:131], v[128:143]\n\t"
    "v_cvt_pk_bf16_f32 v48, v48, v49\n\t"
    "v_cvt_pk_bf16_f32 v49, v50, v51\n\t"
    "v_cvt_pk_bf16_f32 v50, v52, v53\n\t"
    "ds_read_b64_tr_b16 v[200:201], v228 offset:9216\n\t"
    "v_mfma_f32_32x32x16_bf16 v[112:127], a[224:227], a[160:163], v[144:159]\n\t"
    "v_cvt_pk_bf16_f32 v51, v54, v55\n\t"
    "v_add_f32_e32 v234, v234, v54\n\t"
    "v_exp_f32_e32 v40, v40\n\t"
    "ds_read_b64_tr_b16 v[202:203], v228 offset:11264\n\t"
    "ds_read_b64_tr_b16 v[216:217], v228 offset:9728\n\t"
    "v_mfma_f32_32x32x16_bf16 v[96:111], a[228:231], a[132:135], v[96:111]\n\t"
    "v_add_f32_e32 v235, v235, v55\n\t"
    "v_exp_f32_e32 v41, v41\n\t"
    "v_add_f32_e32 v232, v232, v40\n\t"
    "ds_read_b64_tr_b16 v[218:219], v228 offset:11776\n\t"
    "v_mfma_f32_32x32x16_bf16 v[112:127], a[228:231], a[164:167], v[112:127]\n\t"
    "v_exp_f32_e32 v42, v42\n\t"
    "v_add_f32_e32 v233, v233, v41\n\t"
    "v_exp_f32_e32 v43, v43\n\t"
    "ds_read_b64_tr_b16 v[172:173], v228 offset:12288\n\t"
    "v_mfma_f32_32x32x16_bf16 v[96:111], a[232:235], a[136:139], v[96:111]\n\t"
    "v_add_f32_e32 v232, v232, v42\n\t"
    "v_exp_f32_e32 v44, v44\n\t"
    "v_add_f32_e32 v233, v233, v43\n\t"
    "v_exp_f32_e32 v45, v45\n\t"
    "ds_read_b64_tr_b16 v[174:175], v228 offset:14336\n\t"
    "v_mfma_f32_32x32x16_bf16 v[112:127], a[232:235], a[168:171], v[112:127]\n\t"
    "v_add_f32_e32 v232, v232, v44\n\t"
    "v_exp_f32_e32 v46, v46\n\t"
    "v_add_f32_e32 v233, v233, v45\n\t"
    "ds_read_b64_tr_b16 v[188:189], v228 offset:12800\n\t"
    "ds_read_b64_tr_b16 v[190:191], v228 offset:14848\n\t"
    "v_mfma_f32_32x32x16_bf16 v[96:111], a[236:239], a[140:143], v[96:111]\n\t"
    "v_exp_f32_e32 v47, v47\n\t"
    "v_cvt_pk_bf16_f32 v40, v40, v41\n\t"
    "v_cvt_pk_bf16_f32 v41, v42, v43\n\t"
    "ds_read_b64_tr_b16 v[204:205], v228 offset:13312\n\t"
    "v_mfma_f32_32x32x16_bf16 v[112:127], a[236:239], a[172:175], v[112:127]\n\t"
    "v_cvt_pk_bf16_f32 v42, v44, v45\n\t"
    "v_cvt_pk_bf16_f32 v43, v46, v47\n\t"
    "v_add_f32_e32 v232, v232, v46\n\t"
    "ds_read_b64_tr_b16 v[206:207], v228 offset:15360\n\t"
    "v_mfma_f32_32x32x16_bf16 v[96:111], a[240:243], a[144:147], v[96:111]\n\t"
    "v_exp_f32_e32 v56, v56\n\t"
    "v_add_f32_e32 v233, v233, v47\n\t"
    "v_exp_f32_e32 v57, v57\n\t"
    "ds_read_b64_tr_b16 v[220:221], v228 offset:13824\n\t"
    "v_mfma_f32_32x32x16_bf16 v[112:127], a[240:243], a[176:179], v[112:127]\n\t"
    "v_add_f32_e32 v234, v234, v56\n\t"
    "v_exp_f32_e32 v58, v58\n\t"
    "v_add_f32_e32 v235, v235, v57\n\t"
    "ds_read_b64_tr_b16 v[222:223], v228 offset:15872\n\t"
    "v_mfma_f32_32x32x16_bf16 v[96:111], a[244:247], a[148:151], v[96:111]\n\t"
    "v_exp_f32_e32 v59, v59\n\t"
    "v_add_f32_e32 v234, v234, v58\n\t"
    "v_exp_f32_e32 v60, v60\n\t"
    "v_mfma_f32_32x32x16_bf16 v[112:127], a[244:247], a[180:183], v[112:127]\n\t"
    "v_add_f32_e32 v235, v235, v59\n\t"
    "v_exp_f32_e32 v61, v61\n\t"
    "v_add_f32_e32 v234, v234, v60\n\t"
    "v_mfma_f32_32x32x16_bf16 v[96:111], a[248:251], a[152:155], v[96:111]\n\t"
    "v_exp_f32_e32 v62, v62\n\t"
    "v_add_f32_e32 v235, v235, v61\n\t"
    "v_exp_f32_e32 v63, v63\n\t"
    "v_mfma_f32_32x32x16_bf16 v[112:127], a[248:251], a[184:187], v[112:127]\n\t"
    "v_cvt_pk_bf16_f32 v56, v56, v57\n\t"
    "v_cvt_pk_bf16_f32 v57, v58, v59\n\t"
    "v_cvt_pk_bf16_f32 v58, v60, v61\n\t"
    "v_mfma_f32_32x32x16_bf16 v[96:111], a[252:255], a[156:159], v[96:111]\n\t"
    "v_cvt_pk_bf16_f32 v59, v62, v63\n\t"
    "v_add_f32_e32 v234, v234, v62\n\t"
    "v_add_f32_e32 v235, v235, v63\n\t"
    "v_mfma_f32_32x32x16_bf16 v[112:127], a[252:255], a[188:191], v[112:127]\n\t"
    "v_max_f32_e32 v242, v232, v233\n\t"
    "v_max3_f32 v242, v242, v234, v235\n\t"
    "v_cmp_lt_f32_e32 vcc, s54, v242\n\t"
    "s_getpc_b64 s[56:57]\n\t"
    "s_cbranch_vccnz RARE_0_%=\n\t"
    "RET_a1_%=:\n\t"
    "v_add_f32_e32 v236, v236, v232\n\t"
    "v_add_f32_e32 v237, v237, v233\n\t"
    "v_add_f32_e32 v238, v238, v234\n\t"
    "v_add_f32_e32 v239, v239, v235\n\t"
    "s_waitcnt vmcnt(0) lgkmcnt(0)\n\t"
    "s_barrier\n\t"
    "v_mfma_f32_32x32x16_bf16 a[0:15], v[160:163], v[0:3], a[0:15]\n\t"
    "s_mov_b32 m0, s41\n\t"
    "buffer_load_dwordx4 v229, %[srdK], s50 offen offset:0 lds\n\t"
    "v_mfma_f32_32x32x16_bf16 a[16:31], v[160:163], v[16:19], a[16:31]\n\t"
    "buffer_load_dwordx4 v230, %[srdK], s50 offen offset:1024 lds\n\t"
    "v_mfma_f32_32x32x16_bf16 a[32:47], v[176:179], v[0:3], a[32:47]\n\t"
    "buffer_load_dwordx4 v229, %[srdK], s50 offen offset:2048 lds\n\t"
    "v_mfma_f32_32x32x16_bf16 a[48:63], v[176:179], v[16:19], a[48:63]\n\t"
    "buffer_load_dwordx4 v230, %[srdK], s50 offen offset:3072 lds\n\t"
    "v_mfma_f32_32x32x16_bf16 a[64:79], v[192:195], v[0:3], a[64:79]\n\t"
    "s_mov_b32 m0, s42\n\t"
    "buffer_load_dwordx4 v231, %[srdV], s51 offen lds\n\t"
    "v_mfma_f32_32x32x16_bf16 a[80:95], v[192:195], v[16:19], a[80:95]\n\t"
    "s_mov_b32 m0, s43\n\t"
    "s_add_i32 s52, s51, 128\n\t"
    "buffer_load_dwordx4 v231, %[srdV], s52 offen lds\n\t"
    "v_mfma_f32_32x32x16_bf16 a[96:111], v[208:211], v[0:3], a[96:111]\n\t"
    "s_mov_b32 m0, s44\n\t"
    "s_add_i32 s52, s51, 2048\n\t"
    "buffer_load_dwordx4 v231, %[srdV], s52 offen lds\n\t"
    "v_mfma_f32_32x32x16_bf16 a[112:127], v[208:211], v[16:19], a[112:127]\n\t"
    "s_mov_b32 m0, s45\n\t"
    "s_add_i32 s52, s51, 2176\n\t"
    "buffer_load_dwordx4 v231, %[srdV], s52 offen lds\n\t"
    "v_mfma_f32_32x32x16_bf16 a[0:15], v[164:167], v[8:11], a[0:15]\n\t"
    "s_add_i32 s50, s50, 16384\n\t"
    "s_add_i32 s51, s51, 16384\n\t"
    "ds_read_b128 a[192:195], v224 offset:0\n\t"
    "v_mfma_f32_32x32x16_bf16 a[16:31], v[164:167], v[24:27], a[16:31]\n\t"
    "ds_read_b128 a[196:199], v225 offset:0\n\t"
    "v_exp_f32_e32 v64, v64\n\t"
    "v_exp_f32_e32 v65, v65\n\t"
    "v_exp_f32_e32 v66, v66\n\t"
    "v_mfma_f32_32x32x16_bf16 a[32:47], v[180:183], v[8:11], a[32:47]\n\t"
    "ds_read_b128 a[200:203], v226 offset:0\n\t"
    "v_exp_f32_e32 v67, v67\n\t"
    "v_add_f32_e32 v232, v64, v66\n\t"
    "v_exp_f32_e32 v68, v68\n\t"
    "v_mfma_f32_32x32x16_bf16 a[48:63], v[180:183], v[24:27], a[48:63]\n\t"
    "ds_read_b128 a[204:207], v227 offset:0\n\t"
    "v_add_f32_e32 v233, v65, v67\n\t"
    "v_exp_f32_e32 v69, v69\n\t"
    "v_mfma_f32_32x32x16_bf16 a[64:79], v[196:199], v[8:11], a[64:79]\n\t"
    "ds_read_b128 a[208:211], v224 offset:128\n\t"
    "v_add_f32_e32 v232, v232, v68\n\t"
    "v_exp_f32_e32 v70, v70\n\t"
    "v_add_f32_e32 v233, v233, v69\n\t"
    "v_mfma_f32_32x32x16_bf16 a[80:95], v[196:199], v[24:27], a[80:95]\n\t"
    "ds_read_b128 a[212:215], v225 offset:128\n\t"
    "v_exp_f32_e32 v71, v71\n\t"
    "v_add_f32_e32 v232, v232, v70\n\t"
    "v_exp_f32_e32 v80, v80\n\t"
    "v_mfma_f32_32x32x16_bf16 a[96:111], v[212:215], v[8:11], a[96:111]\n\t"
    "ds_read_b128 a[216:219], v226 offset:128\n\t"
    "v_add_f32_e32 v233, v233, v71\n\t"
    "v_exp_f32_e32 v81, v81\n\t"
    "v_mfma_f32_32x32x16_bf16 a[112:127], v[212:215], v[24:27], a[112:127]\n\t"
    "ds_read_b128 a[220:223], v227 offset:128\n\t"
    "v_exp_f32_e32 v82, v82\n\t"
    "v_exp_f32_e32 v83, v83\n\t"
    "v_add_f32_e32 v234, v80, v82\n\t"
    "v_mfma_f32_32x32x16_bf16 a[0:15], v[168:171], v[32:35], a[0:15]\n\t"
    "ds_read_b128 a[224:227], v224 offset:8192\n\t"
    "v_exp_f32_e32 v84, v84\n\t"
    "v_add_f32_e32 v235, v81, v83\n\t"
    "v_mfma_f32_32x32x16_bf16 a[16:31], v[168:171], v[48:51], a[16:31]\n\t"
    "ds_read_b128 a[228:231], v225 offset:8192\n\t"
    "v_exp_f32_e32 v85, v85\n\t"
    "v_add_f32_e32 v234, v234, v84\n\t"
    "v_exp_f32_e32 v86, v86\n\t"
    "v_mfma_f32_32x32x16_bf16 a[32:47], v[184:187], v[32:35], a[32:47]\n\t"
    "ds_read_b128 a[232:235], v226 offset:8192\n\t"
    "v_add_f32_e32 v235, v235, v85\n\t"
    "v_exp_f32_e32 v87, v87\n\t"
    "v_add_f32_e32 v234, v234, v86\n\t"
    "v_mfma_f32_32x32x16_bf16 a[48:63], v[184:187], v[48:51], a[48:63]\n\t"
    "ds_read_b128 a[236:239], v227 offset:8192\n\t"
    "v_exp_f32_e32 v72, v72\n\t"
    "v_add_f32_e32 v235, v235, v87\n\t"
    "v_mfma_f32_32x32x16_bf16 a[64:79], v[200:203], v[32:35], a[64:79]\n\t"
    "ds_read_b128 a[240:243], v224 offset:8320\n\t"
    "v_exp_f32_e32 v73, v73\n\t"
    "v_add_f32_e32 v232, v232, v72\n\t"
    "v_exp_f32_e32 v74, v74\n\t"
    "v_mfma_f32_32x32x16_bf16 a[80:95], v[200:203], v[48:51], a[80:95]\n\t"
    "ds_read_b128 a[244:247], v225 offset:8320\n\t"
    "v_add_f32_e32 v233, v233, v73\n\t"
    "v_exp_f32_e32 v75, v75\n\t"
    "v_mfma_f32_32x32x16_bf16 a[96:111], v[216:219], v[32:35], a[96:111]\n\t"
    "ds_read_b128 a[248:251], v226 offset:8320\n\t"
    "v_add_f32_e32 v232, v232, v74\n\t"
    "v_exp_f32_e32 v76, v76\n\t"
    "v_add_f32_e32 v233, v233, v75\n\t"
    "v_mfma_f32_32x32x16_bf16 a[112:127], v[216:219], v[48:51], a[112:127]\n\t"
    "ds_read_b128 a[252:255], v227 offset:8320\n\t"
    "v_exp_f32_e32 v77, v77\n\t"
    "v_add_f32_e32 v232, v232, v76\n\t"
    "v_exp_f32_e32 v78, v78\n\t"
    "v_mfma_f32_32x32x16_bf16 a[0:15], v[172:175], v[40:43], a[0:15]\n\t"
    "v_add_f32_e32 v233, v233, v77\n\t"
    "v_exp_f32_e32 v79, v79\n\t"
    "v_mfma_f32_32x32x16_bf16 a[16:31], v[172:175], v[56:59], a[16:31]\n\t"
    "v_add_f32_e32 v232, v232, v78\n\t"
    "v_exp_f32_e32 v88, v88\n\t"
    "v_add_f32_e32 v233, v233, v79\n\t"
    "v_mfma_f32_32x32x16_bf16 a[32:47], v[188:191], v[40:43], a[32:47]\n\t"
    "v_exp_f32_e32 v89, v89\n\t"
    "v_add_f32_e32 v234, v234, v88\n\t"
    "v_mfma_f32_32x32x16_bf16 a[48:63], v[188:191], v[56:59], a[48:63]\n\t"
    "v_exp_f32_e32 v90, v90\n\t"
    "v_add_f32_e32 v235, v235, v89\n\t"
    "v_exp_f32_e32 v91, v91\n\t"
    "v_mfma_f32_32x32x16_bf16 a[64:79], v[204:207], v[40:43], a[64:79]\n\t"
    "v_add_f32_e32 v234, v234, v90\n\t"
    "v_exp_f32_e32 v92, v92\n\t"
    "v_add_f32_e32 v235, v235, v91\n\t"
    "v_mfma_f32_32x32x16_bf16 a[80:95], v[204:207], v[56:59], a[80:95]\n\t"
    "v_exp_f32_e32 v93, v93\n\t"
    "v_add_f32_e32 v234, v234, v92\n\t"
    "v_mfma_f32_32x32x16_bf16 a[96:111], v[220:223], v[40:43], a[96:111]\n\t"
    "v_exp_f32_e32 v94, v94\n\t"
    "v_add_f32_e32 v235, v235, v93\n\t"
    "v_exp_f32_e32 v95, v95\n\t"
    "v_mfma_f32_32x32x16_bf16 a[112:127], v[220:223], v[56:59], a[112:127]\n\t"
    "v_add_f32_e32 v234, v234, v94\n\t"
    "v_add_f32_e32 v235, v235, v95\n\t"
    "s_waitcnt lgkmcnt(0)\n\t"
    "v_mfma_f32_32x32x16_bf16 v[0:15], a[192:195], a[128:131], v[128:143]\n\t"
    "v_cvt_pk_bf16_f32 v64, v64, v65\n\t"
    "v_cvt_pk_bf16_f32 v65, v66, v67\n\t"
    "v_cvt_pk_bf16_f32 v66, v68, v69\n\t"
    "v_cvt_pk_bf16_f32 v67, v70, v71\n\t"
    "ds_read_b64_tr_b16 v[160:161], v228 offset:16384\n\t"
    "ds_read_b64_tr_b16 v[162:163], v228 offset:18432\n\t"
    "v_mfma_f32_32x32x16_bf16 v[16:31], a[192:195], a[160:163], v[144:159]\n\t"
    "v_cvt_pk_bf16_f32 v80, v80, v81\n\t"
    "v_cvt_pk_bf16_f32 v81, v82, v83\n\t"
    "v_cvt_pk_bf16_f32 v82, v84, v85\n\t"
    "ds_read_b64_tr_b16 v[176:177], v228 offset:16896\n\t"
    "v_mfma_f32_32x32x16_bf16 v[0:15], a[196:199], a[132:135], v[0:15]\n\t"
    "v_cvt_pk_bf16_f32 v83, v86, v87\n\t"
    "v_cvt_pk_bf16_f32 v72, v72, v73\n\t"
    "v_cvt_pk_bf16_f32 v73, v74, v75\n\t"
    "ds_read_b64_tr_b16 v[178:179], v228 offset:18944\n\t"
    "v_mfma_f32_32x32x16_bf16 v[16:31], a[196:199], a[164:167], v[16:31]\n\t"
    "v_cvt_pk_bf16_f32 v74, v76, v77\n\t"
    "v_cvt_pk_bf16_f32 v75, v78, v79\n\t"
    "v_cvt_pk_bf16_f32 v88, v88, v89\n\t"
    "ds_read_b64_tr_b16 v[192:193], v228 offset:17408\n\t"
    "v_mfma_f32_32x32x16_bf16 v[0:15], a[200:203], a[136:139], v[0:15]\n\t"
    "v_cvt_pk_bf16_f32 v89, v90, v91\n\t"
    "v_cvt_pk_bf16_f32 v90, v92, v93\n\t"
    "v_cvt_pk_bf16_f32 v91, v94, v95\n\t"
    "ds_read_b64_tr_b16 v[194:195], v228 offset:19456\n\t"
    "ds_read_b64_tr_b16 v[208:209], v228 offset:17920\n\t"
    "v_mfma_f32_32x32x16_bf16 v[16:31], a[200:203], a[168:171], v[16:31]\n\t"
    "v_exp_f32_e32 v96, v96\n\t"
    "v_exp_f32_e32 v97, v97\n\t"
    "v_add_f32_e32 v232, v232, v96\n\t"
    "ds_read_b64_tr_b16 v[210:211], v228 offset:19968\n\t"
    "v_mfma_f32_32x32x16_bf16 v[0:15], a[204:207], a[140:143], v[0:15]\n\t"
    "v_exp_f32_e32 v98, v98\n\t"
    "v_add_f32_e32 v233, v233, v97\n\t"
    "v_exp_f32_e32 v99, v99\n\t"
    "ds_read_b64_tr_b16 v[164:165], v228 offset:20480\n\t"
    "v_mfma_f32_32x32x16_bf16 v[16:31], a[204:207], a[172:175], v[16:31]\n\t"
    "v_add_f32_e32 v232, v232, v98\n\t"
    "v_exp_f32_e32 v100, v100\n\t"
    "v_add_f32_e32 v233, v233, v99\n\t"
    "ds_read_b64_tr_b16 v[166:167], v228 offset:22528\n\t"
    "v_mfma_f32_32x32x16_bf16 v[0:15], a[208:211], a[144:147], v[0:15]\n\t"
    "v_exp_f32_e32 v101, v101\n\t"
    "v_add_f32_e32 v232, v232, v100\n\t"
    "v_exp_f32_e32 v102, v102\n\t"
    "ds_read_b64_tr_b16 v[180:181], v228 offset:20992\n\t"
    "ds_read_b64_tr_b16 v[182:183], v228 offset:23040\n\t"
    "v_mfma_f32_32x32x16_bf16 v[16:31], a[208:211], a[176:179], v[16:31]\n\t"
    "v_add_f32_e32 v233, v233, v101\n\t"
    "v_exp_f32_e32 v103, v103\n\t"
    "v_cvt_pk_bf16_f32 v96, v96, v97\n\t"
    "ds_read_b64_tr_b16 v[196:197], v228 offset:21504\n\t"
    "v_mfma_f32_32x32x16_bf16 v[0:15], a[212:215], a[148:151], v[0:15]\n\t"
    "v_cvt_pk_bf16_f32 v97, v98, v99\n\t"
    "v_cvt_pk_bf16_f32 v98, v100, v101\n\t"
    "v_cvt_pk_bf16_f32 v99, v102, v103\n\t"
    "v_add_f32_e32 v232, v232, v102\n\t"
    "ds_read_b64_tr_b16 v[198:199], v228 offset:23552\n\t"
    "v_mfma_f32_32x32x16_bf16 v[16:31], a[212:215], a[180:183], v[16:31]\n\t"
    "v_exp_f32_e32 v112, v112\n\t"
    "v_add_f32_e32 v233, v233, v103\n\t"
    "v_exp_f32_e32 v113, v113\n\t"
    "ds_read_b64_tr_b16 v[212:213], v228 offset:22016\n\t"
    "v_mfma_f32_32x32x16_bf16 v[0:15], a[216:219], a[152:155], v[0:15]\n\t"
    "v_add_f32_e32 v234, v234, v112\n\t"
    "v_exp_f32_e32 v114, v114\n\t"
    "v_add_f32_e32 v235, v235, v113\n\t"
    "ds_read_b64_tr_b16 v[214:215], v228 offset:24064\n\t"
    "v_mfma_f32_32x32x16_bf16 v[16:31], a[216:219], a[184:187], v[16:31]\n\t"
    "v_exp_f32_e32 v115, v115\n\t"
    "v_add_f32_e32 v234, v234, v114\n\t"
    "v_exp_f32_e32 v116, v116\n\t"
    "ds_read_b64_tr_b16 v[168:169], v228 offset:24576\n\t"
    "ds_read_b64_tr_b16 v[170:171], v228 offset:26624\n\t"
    "v_mfma_f32_32x32x16_bf16 v[0:15], a[220:223], a[156:159], v[0:15]\n\t"
    "v_add_f32_e32 v235, v235, v115\n\t"
    "v_exp_f32_e32 v117, v117\n\t"
    "v_add_f32_e32 v234, v234, v116\n\t"
    "ds_read_b64_tr_b16 v[184:185], v228 offset:25088\n\t"
    "v_mfma_f32_32x32x16_bf16 v[16:31], a[220:223], a[188:191], v[16:31]\n\t"
    "v_exp_f32_e32 v118, v118\n\t"
    "v_add_f32_e32 v235, v235, v117\n\t"
    "v_exp_f32_e32 v119, v119\n\t"
    "ds_read_b64_tr_b16 v[186:187], v228 offset:27136\n\t"
    "v_mfma_f32_32x32x16_bf16 v[32:47], a[224:227], a[128:131], v[128:143]\n\t"
    "v_cvt_pk_bf16_f32 v112, v112, v113\n\t"
    "v_cvt_pk_bf16_f32 v113, v114, v115\n\t"
    "v_cvt_pk_bf16_f32 v114, v116, v117\n\t"
    "ds_read_b64_tr_b16 v[200:201], v228 offset:25600\n\t"
    "v_mfma_f32_32x32x16_bf16 v[48:63], a[224:227], a[160:163], v[144:159]\n\t"
    "v_cvt_pk_bf16_f32 v115, v118, v119\n\t"
    "v_add_f32_e32 v234, v234, v118\n\t"
    "v_exp_f32_e32 v104, v104\n\t"
    "ds_read_b64_tr_b16 v[202:203], v228 offset:27648\n\t"
    "ds_read_b64_tr_b16 v[216:217], v228 offset:26112\n\t"
    "v_mfma_f32_32x32x16_bf16 v[32:47], a[228:231], a[132:135], v[32:47]\n\t"
    "v_add_f32_e32 v235, v235, v119\n\t"
    "v_exp_f32_e32 v105, v105\n\t"
    "v_add_f32_e32 v232, v232, v104\n\t"
    "ds_read_b64_tr_b16 v[218:219], v228 offset:28160\n\t"
    "v_mfma_f32_32x32x16_bf16 v[48:63], a[228:231], a[164:167], v[48:63]\n\t"
    "v_exp_f32_e32 v106, v106\n\t"
    "v_add_f32_e32 v233, v233, v105\n\t"
    "v_exp_f32_e32 v107, v107\n\t"
    "ds_read_b64_tr_b16 v[172:173], v228 offset:28672\n\t"
    "v_mfma_f32_32x32x16_bf16 v[32:47], a[232:235], a[136:139], v[32:47]\n\t"
    "v_add_f32_e32 v232, v232, v106\n\t"
    "v_exp_f32_e32 v108, v108\n\t"
    "v_add_f32_e32 v233, v233, v107\n\t"
    "v_exp_f32_e32 v109, v109\n\t"
    "ds_read_b64_tr_b16 v[174:175], v228 offset:30720\n\t"
    "v_mfma_f32_32x32x16_bf16 v[48:63], a[232:235], a[168:171], v[48:63]\n\t"
    "v_add_f32_e32 v232, v232, v108\n\t"
    "v_exp_f32_e32 v110, v110\n\t"
    "v_add_f32_e32 v233, v233, v109\n\t"
    "ds_read_b64_tr_b16 v[188:189], v228 offset:29184\n\t"
    "ds_read_b64_tr_b16 v[190:191], v228 offset:31232\n\t"
    "v_mfma_f32_32x32x16_bf16 v[32:47], a[236:239], a[140:143], v[32:47]\n\t"
    "v_exp_f32_e32 v111, v111\n\t"
    "v_cvt_pk_bf16_f32 v104, v104, v105\n\t"
    "v_cvt_pk_bf16_f32 v105, v106, v107\n\t"
    "ds_read_b64_tr_b16 v[204:205], v228 offset:29696\n\t"
    "v_mfma_f32_32x32x16_bf16 v[48:63], a[236:239], a[172:175], v[48:63]\n\t"
    "v_cvt_pk_bf16_f32 v106, v108, v109\n\t"
    "v_cvt_pk_bf16_f32 v107, v110, v111\n\t"
    "v_add_f32_e32 v232, v232, v110\n\t"
    "ds_read_b64_tr_b16 v[206:207], v228 offset:31744\n\t"
    "v_mfma_f32_32x32x16_bf16 v[32:47], a[240:243], a[144:147], v[32:47]\n\t"
    "v_exp_f32_e32 v120, v120\n\t"
    "v_add_f32_e32 v233, v233, v111\n\t"
    "v_exp_f32_e32 v121, v121\n\t"
    "ds_read_b64_tr_b16 v[220:221], v228 offset:30208\n\t"
    "v_mfma_f32_32x32x16_bf16 v[48:63], a[240:243], a[176:179], v[48:63]\n\t"
    "v_add_f32_e32 v234, v234, v120\n\t"
    "v_exp_f32_e32 v122, v122\n\t"
    "v_add_f32_e32 v235, v235, v121\n\t"
    "ds_read_b64_tr_b16 v[222:223], v228 offset:32256\n\t"
    "v_mfma_f32_32x32x16_bf16 v[32:47], a[244:247], a[148:151], v[32:47]\n\t"
    "v_exp_f32_e32 v123, v123\n\t"
    "v_add_f32_e32 v234, v234, v122\n\t"
    "v_exp_f32_e32 v124, v124\n\t"
    "v_mfma_f32_32x32x16_bf16 v[48:63], a[244:247], a[180:183], v[48:63]\n\t"
    "v_add_f32_e32 v235, v235, v123\n\t"
    "v_exp_f32_e32 v125, v125\n\t"
    "v_add_f32_e32 v234, v234, v124\n\t"
    "v_mfma_f32_32x32x16_bf16 v[32:47], a[248:251], a[152:155], v[32:47]\n\t"
    "v_exp_f32_e32 v126, v126\n\t"
    "v_add_f32_e32 v235, v235, v125\n\t"
    "v_exp_f32_e32 v127, v127\n\t"
    "v_mfma_f32_32x32x16_bf16 v[48:63], a[248:251], a[184:187], v[48:63]\n\t"
    "v_cvt_pk_bf16_f32 v120, v120, v121\n\t"
    "v_cvt_pk_bf16_f32 v121, v122, v123\n\t"
    "v_cvt_pk_bf16_f32 v122, v124, v125\n\t"
    "v_mfma_f32_32x32x16_bf16 v[32:47], a[252:255], a[156:159], v[32:47]\n\t"
    "v_cvt_pk_bf16_f32 v123, v126, v127\n\t"
    "v_add_f32_e32 v234, v234, v126\n\t"
    "v_add_f32_e32 v235, v235, v127\n\t"
    "v_mfma_f32_32x32x16_bf16 v[48:63], a[252:255], a[188:191], v[48:63]\n\t"
    "v_max_f32_e32 v242, v232, v233\n\t"
    "v_max3_f32 v242, v242, v234, v235\n\t"
    "v_cmp_lt_f32_e32 vcc, s54, v242\n\t"
    "s_getpc_b64 s[56:57]\n\t"
    "s_cbranch_vccnz RARE_1_%=\n\t"
    "RET_a0_%=:\n\t"
    "v_add_f32_e32 v236, v236, v232\n\t"
    "v_add_f32_e32 v237, v237, v233\n\t"
    "v_add_f32_e32 v238, v238, v234\n\t"
    "v_add_f32_e32 v239, v239, v235\n\t"
    "s_waitcnt vmcnt(0) lgkmcnt(0)\n\t"
    "s_barrier\n\t"
    "v_mfma_f32_32x32x16_bf16 a[0:15], v[160:163], v[64:67], a[0:15]\n\t"
    "s_mov_b32 m0, s40\n\t"
    "buffer_load_dwordx4 v229, %[srdK], s50 offen offset:0 lds\n\t"
    "v_mfma_f32_32x32x16_bf16 a[16:31], v[160:163], v[80:83], a[16:31]\n\t"
    "buffer_load_dwordx4 v230, %[srdK], s50 offen offset:1024 lds\n\t"
    "v_mfma_f32_32x32x16_bf16 a[32:47], v[176:179], v[64:67], a[32:47]\n\t"
    "buffer_load_dwordx4 v229, %[srdK], s50 offen offset:2048 lds\n\t"
    "v_mfma_f32_32x32x16_bf16 a[48:63], v[176:179], v[80:83], a[48:63]\n\t"
    "buffer_load_dwordx4 v230, %[srdK], s50 offen offset:3072 lds\n\t"
    "v_mfma_f32_32x32x16_bf16 a[64:79], v[192:195], v[64:67], a[64:79]\n\t"
    "s_mov_b32 m0, s46\n\t"
    "buffer_load_dwordx4 v231, %[srdV], s51 offen lds\n\t"
    "v_mfma_f32_32x32x16_bf16 a[80:95], v[192:195], v[80:83], a[80:95]\n\t"
    "s_mov_b32 m0, s47\n\t"
    "s_add_i32 s52, s51, 128\n\t"
    "buffer_load_dwordx4 v231, %[srdV], s52 offen lds\n\t"
    "v_mfma_f32_32x32x16_bf16 a[96:111], v[208:211], v[64:67], a[96:111]\n\t"
    "s_mov_b32 m0, s48\n\t"
    "s_add_i32 s52, s51, 2048\n\t"
    "buffer_load_dwordx4 v231, %[srdV], s52 offen lds\n\t"
    "v_mfma_f32_32x32x16_bf16 a[112:127], v[208:211], v[80:83], a[112:127]\n\t"
    "s_mov_b32 m0, s49\n\t"
    "s_add_i32 s52, s51, 2176\n\t"
    "buffer_load_dwordx4 v231, %[srdV], s52 offen lds\n\t"
    "v_mfma_f32_32x32x16_bf16 a[0:15], v[164:167], v[72:75], a[0:15]\n\t"
    "s_add_i32 s50, s50, 16384\n\t"
    "s_add_i32 s51, s51, 16384\n\t"
    "ds_read_b128 a[192:195], v224 offset:16384\n\t"
    "v_mfma_f32_32x32x16_bf16 a[16:31], v[164:167], v[88:91], a[16:31]\n\t"
    "ds_read_b128 a[196:199], v225 offset:16384\n\t"
    "v_exp_f32_e32 v0, v0\n\t"
    "v_exp_f32_e32 v1, v1\n\t"
    "v_exp_f32_e32 v2, v2\n\t"
    "v_mfma_f32_32x32x16_bf16 a[32:47], v[180:183], v[72:75], a[32:47]\n\t"
    "ds_read_b128 a[200:203], v226 offset:16384\n\t"
    "v_exp_f32_e32 v3, v3\n\t"
    "v_add_f32_e32 v232, v0, v2\n\t"
    "v_exp_f32_e32 v4, v4\n\t"
    "v_mfma_f32_32x32x16_bf16 a[48:63], v[180:183], v[88:91], a[48:63]\n\t"
    "ds_read_b128 a[204:207], v227 offset:16384\n\t"
    "v_add_f32_e32 v233, v1, v3\n\t"
    "v_exp_f32_e32 v5, v5\n\t"
    "v_mfma_f32_32x32x16_bf16 a[64:79], v[196:199], v[72:75], a[64:79]\n\t"
    "ds_read_b128 a[208:211], v224 offset:16512\n\t"
    "v_add_f32_e32 v232, v232, v4\n\t"
    "v_exp_f32_e32 v6, v6\n\t"
    "v_add_f32_e32 v233, v233, v5\n\t"
    "v_mfma_f32_32x32x16_bf16 a[80:95], v[196:199], v[88:91], a[80:95]\n\t"
    "ds_read_b128 a[212:215], v225 offset:16512\n\t"
    "v_exp_f32_e32 v7, v7\n\t"
    "v_add_f32_e32 v232, v232, v6\n\t"
    "v_exp_f32_e32 v16, v16\n\t"
    "v_mfma_f32_32x32x16_bf16 a[96:111], v[212:215], v[72:75], a[96:111]\n\t"
    "ds_read_b128 a[216:219], v226 offset:16512\n\t"
    "v_add_f32_e32 v233, v233, v7\n\t"
    "v_exp_f32_e32 v17, v17\n\t"
    "v_mfma_f32_32x32x16_bf16 a[112:127], v[212:215], v[88:91], a[112:127]\n\t"
    "ds_read_b128 a[220:223], v227 offset:16512\n\t"
    "v_exp_f32_e32 v18, v18\n\t"
    "v_exp_f32_e32 v19, v19\n\t"
    "v_add_f32_e32 v234, v16, v18\n\t"
    "v_mfma_f32_32x32x16_bf16 a[0:15], v[168:171], v[96:99], a[0:15]\n\t"
    "ds_read_b128 a[224:227], v224 offset:24576\n\t"
    "v_exp_f32_e32 v20, v20\n\t"
    "v_add_f32_e32 v235, v17, v19\n\t"
    "v_mfma_f32_32x32x16_bf16 a[16:31], v[168:171], v[112:115], a[16:31]\n\t"
    "ds_read_b128 a[228:231], v225 offset:24576\n\t"
    "v_exp_f32_e32 v21, v21\n\t"
    "v_add_f32_e32 v234, v234, v20\n\t"
    "v_exp_f32_e32 v22, v22\n\t"
    "v_mfma_f32_32x32x16_bf16 a[32:47], v[184:187], v[96:99], a[32:47]\n\t"
    "ds_read_b128 a[232:235], v226 offset:24576\n\t"
    "v_add_f32_e32 v235, v235, v21\n\t"
    "v_exp_f32_e32 v23, v23\n\t"
    "v_add_f32_e32 v234, v234, v22\n\t"
    "v_mfma_f32_32x32x16_bf16 a[48:63], v[184:187], v[112:115], a[48:63]\n\t"
    "ds_read_b128 a[236:239], v227 offset:24576\n\t"
    "v_exp_f32_e32 v8, v8\n\t"
    "v_add_f32_e32 v235, v235, v23\n\t"
    "v_mfma_f32_32x32x16_bf16 a[64:79], v[200:203], v[96:99], a[64:79]\n\t"
    "ds_read_b128 a[240:243], v224 offset:24704\n\t"
    "v_exp_f32_e32 v9, v9\n\t"
    "v_add_f32_e32 v232, v232, v8\n\t"
    "v_exp_f32_e32 v10, v10\n\t"
    "v_mfma_f32_32x32x16_bf16 a[80:95], v[200:203], v[112:115], a[80:95]\n\t"
    "ds_read_b128 a[244:247], v225 offset:24704\n\t"
    "v_add_f32_e32 v233, v233, v9\n\t"
    "v_exp_f32_e32 v11, v11\n\t"
    "v_mfma_f32_32x32x16_bf16 a[96:111], v[216:219], v[96:99], a[96:111]\n\t"
    "ds_read_b128 a[248:251], v226 offset:24704\n\t"
    "v_add_f32_e32 v232, v232, v10\n\t"
    "v_exp_f32_e32 v12, v12\n\t"
    "v_add_f32_e32 v233, v233, v11\n\t"
    "v_mfma_f32_32x32x16_bf16 a[112:127], v[216:219], v[112:115], a[112:127]\n\t"
    "ds_read_b128 a[252:255], v227 offset:24704\n\t"
    "v_exp_f32_e32 v13, v13\n\t"
    "v_add_f32_e32 v232, v232, v12\n\t"
    "v_exp_f32_e32 v14, v14\n\t"
    "v_mfma_f32_32x32x16_bf16 a[0:15], v[172:175], v[104:107], a[0:15]\n\t"
    "v_add_f32_e32 v233, v233, v13\n\t"
    "v_exp_f32_e32 v15, v15\n\t"
    "v_mfma_f32_32x32x16_bf16 a[16:31], v[172:175], v[120:123], a[16:31]\n\t"
    "v_add_f32_e32 v232, v232, v14\n\t"
    "v_exp_f32_e32 v24, v24\n\t"
    "v_add_f32_e32 v233, v233, v15\n\t"
    "v_mfma_f32_32x32x16_bf16 a[32:47], v[188:191], v[104:107], a[32:47]\n\t"
    "v_exp_f32_e32 v25, v25\n\t"
    "v_add_f32_e32 v234, v234, v24\n\t"
    "v_mfma_f32_32x32x16_bf16 a[48:63], v[188:191], v[120:123], a[48:63]\n\t"
    "v_exp_f32_e32 v26, v26\n\t"
    "v_add_f32_e32 v235, v235, v25\n\t"
    "v_exp_f32_e32 v27, v27\n\t"
    "v_mfma_f32_32x32x16_bf16 a[64:79], v[204:207], v[104:107], a[64:79]\n\t"
    "v_add_f32_e32 v234, v234, v26\n\t"
    "v_exp_f32_e32 v28, v28\n\t"
    "v_add_f32_e32 v235, v235, v27\n\t"
    "v_mfma_f32_32x32x16_bf16 a[80:95], v[204:207], v[120:123], a[80:95]\n\t"
    "v_exp_f32_e32 v29, v29\n\t"
    "v_add_f32_e32 v234, v234, v28\n\t"
    "v_mfma_f32_32x32x16_bf16 a[96:111], v[220:223], v[104:107], a[96:111]\n\t"
    "v_exp_f32_e32 v30, v30\n\t"
    "v_add_f32_e32 v235, v235, v29\n\t"
    "v_exp_f32_e32 v31, v31\n\t"
    "v_mfma_f32_32x32x16_bf16 a[112:127], v[220:223], v[120:123], a[112:127]\n\t"
    "v_add_f32_e32 v234, v234, v30\n\t"
    "v_add_f32_e32 v235, v235, v31\n\t"
    "s_waitcnt lgkmcnt(0)\n\t"
    "s_sub_i32 s53, s53, 1\n\t"
    "s_cmp_lg_u32 s53, 0\n\t"
    "s_cbranch_scc1 LOOP_%=\n\t"
    "v_mfma_f32_32x32x16_bf16 v[64:79], a[192:195], a[128:131], v[128:143]\n\t"
    "v_cvt_pk_bf16_f32 v0, v0, v1\n\t"
    "v_cvt_pk_bf16_f32 v1, v2, v3\n\t"
    "v_cvt_pk_bf16_f32 v2, v4, v5\n\t"
    "v_cvt_pk_bf16_f32 v3, v6, v7\n\t"
    "ds_read_b64_tr_b16 v[160:161], v228 offset:0\n\t"
    "ds_read_b64_tr_b16 v[162:163], v228 offset:2048\n\t"
    "v_mfma_f32_32x32x16_bf16 v[80:95], a[192:195], a[160:163], v[144:159]\n\t"
    "v_cvt_pk_bf16_f32 v16, v16, v17\n\t"
    "v_cvt_pk_bf16_f32 v17, v18, v19\n\t"
    "v_cvt_pk_bf16_f32 v18, v20, v21\n\t"
    "ds_read_b64_tr_b16 v[176:177], v228 offset:512\n\t"
    "v_mfma_f32_32x32x16_bf16 v[64:79], a[196:199], a[132:135], v[64:79]\n\t"
    "v_cvt_pk_bf16_f32 v19, v22, v23\n\t"
    "v_cvt_pk_bf16_f32 v8, v8, v9\n\t"
    "v_cvt_pk_bf16_f32 v9, v10, v11\n\t"
    "ds_read_b64_tr_b16 v[178:179], v228 offset:2560\n\t"
    "v_mfma_f32_32x32x16_bf16 v[80:95], a[196:199], a[164:167], v[80:95]\n\t"
    "v_cvt_pk_bf16_f32 v10, v12, v13\n\t"
    "v_cvt_pk_bf16_f32 v11, v14, v15\n\t"
    "v_cvt_pk_bf16_f32 v24, v24, v25\n\t"
    "ds_read_b64_tr_b16 v[192:193], v228 offset:1024\n\t"
    "v_mfma_f32_32x32x16_bf16 v[64:79], a[200:203], a[136:139], v[64:79]\n\t"
    "v_cvt_pk_bf16_f32 v25, v26, v27\n\t"
    "v_cvt_pk_bf16_f32 v26, v28, v29\n\t"
    "v_cvt_pk_bf16_f32 v27, v30, v31\n\t"
    "ds_read_b64_tr_b16 v[194:195], v228 offset:3072\n\t"
    "ds_read_b64_tr_b16 v[208:209], v228 offset:1536\n\t"
    "v_mfma_f32_32x32x16_bf16 v[80:95], a[200:203], a[168:171], v[80:95]\n\t"
    "v_exp_f32_e32 v32, v32\n\t"
    "v_exp_f32_e32 v33, v33\n\t"
    "v_add_f32_e32 v232, v232, v32\n\t"
    "ds_read_b64_tr_b16 v[210:211], v228 offset:3584\n\t"
    "v_mfma_f32_32x32x16_bf16 v[64:79], a[204:207], a[140:143], v[64:79]\n\t"
    "v_exp_f32_e32 v34, v34\n\t"
    "v_add_f32_e32 v233, v233, v33\n\t"
    "v_exp_f32_e32 v35, v35\n\t"
    "ds_read_b64_tr_b16 v[164:165], v228 offset:4096\n\t"
    "v_mfma_f32_32x32x16_bf16 v[80:95], a[204:207], a[172:175], v[80:95]\n\t"
    "v_add_f32_e32 v232, v232, v34\n\t"
    "v_exp_f32_e32 v36, v36\n\t"
    "v_add_f32_e32 v233, v233, v35\n\t"
    "ds_read_b64_tr_b16 v[166:167], v228 offset:6144\n\t"
    "v_mfma_f32_32x32x16_bf16 v[64:79], a[208:211], a[144:147], v[64:79]\n\t"
    "v_exp_f32_e32 v37, v37\n\t"
    "v_add_f32_e32 v232, v232, v36\n\t"
    "v_exp_f32_e32 v38, v38\n\t"
    "ds_read_b64_tr_b16 v[180:181], v228 offset:4608\n\t"
    "ds_read_b64_tr_b16 v[182:183], v228 offset:6656\n\t"
    "v_mfma_f32_32x32x16_bf16 v[80:95], a[208:211], a[176:179], v[80:95]\n\t"
    "v_add_f32_e32 v233, v233, v37\n\t"
    "v_exp_f32_e32 v39, v39\n\t"
    "v_cvt_pk_bf16_f32 v32, v32, v33\n\t"
    "ds_read_b64_tr_b16 v[196:197], v228 offset:5120\n\t"
    "v_mfma_f32_32x32x16_bf16 v[64:79], a[212:215], a[148:151], v[64:79]\n\t"
    "v_cvt_pk_bf16_f32 v33, v34, v35\n\t"
    "v_cvt_pk_bf16_f32 v34, v36, v37\n\t"
    "v_cvt_pk_bf16_f32 v35, v38, v39\n\t"
    "v_add_f32_e32 v232, v232, v38\n\t"
    "ds_read_b64_tr_b16 v[198:199], v228 offset:7168\n\t"
    "v_mfma_f32_32x32x16_bf16 v[80:95], a[212:215], a[180:183], v[80:95]\n\t"
    "v_exp_f32_e32 v48, v48\n\t"
    "v_add_f32_e32 v233, v233, v39\n\t"
    "v_exp_f32_e32 v49, v49\n\t"
    "ds_read_b64_tr_b16 v[212:213], v228 offset:5632\n\t"
    "v_mfma_f32_32x32x16_bf16 v[64:79], a[216:219], a[152:155], v[64:79]\n\t"
    "v_add_f32_e32 v234, v234, v48\n\t"
    "v_exp_f32_e32 v50, v50\n\t"
    "v_add_f32_e32 v235, v235, v49\n\t"
    "ds_read_b64_tr_b16 v[214:215], v228 offset:7680\n\t"
    "v_mfma_f32_32x32x16_bf16 v[80:95], a[216:219], a[184:187], v[80:95]\n\t"
    "v_exp_f32_e32 v51, v51\n\t"
    "v_add_f32_e32 v234, v234, v50\n\t"
    "v_exp_f32_e32 v52, v52\n\t"
    "ds_read_b64_tr_b16 v[168:169], v228 offset:8192\n\t"
    "ds_read_b64_tr_b16 v[170:171], v228 offset:10240\n\t"
    "v_mfma_f32_32x32x16_bf16 v[64:79], a[220:223], a[156:159], v[64:79]\n\t"
    "v_add_f32_e32 v235, v235, v51\n\t"
    "v_exp_f32_e32 v53, v53\n\t"
    "v_add_f32_e32 v234, v234, v52\n\t"
    "ds_read_b64_tr_b16 v[184:185], v228 offset:8704\n\t"
    "v_mfma_f32_32x32x16_bf16 v[80:95], a[220:223], a[188:191], v[80:95]\n\t"
    "v_exp_f32_e32 v54, v54\n\t"
    "v_add_f32_e32 v235, v235, v53\n\t"
    "v_exp_f32_e32 v55, v55\n\t"
    "ds_read_b64_tr_b16 v[186:187], v228 offset:10752\n\t"
    "v_mfma_f32_32x32x16_bf16 v[96:111], a[224:227], a[128:131], v[128:143]\n\t"
    "v_cvt_pk_bf16_f32 v48, v48, v49\n\t"
    "v_cvt_pk_bf16_f32 v49, v50, v51\n\t"
    "v_cvt_pk_bf16_f32 v50, v52, v53\n\t"
    "ds_read_b64_tr_b16 v[200:201], v228 offset:9216\n\t"
    "v_mfma_f32_32x32x16_bf16 v[112:127], a[224:227], a[160:163], v[144:159]\n\t"
    "v_cvt_pk_bf16_f32 v51, v54, v55\n\t"
    "v_add_f32_e32 v234, v234, v54\n\t"
    "v_exp_f32_e32 v40, v40\n\t"
    "ds_read_b64_tr_b16 v[202:203], v228 offset:11264\n\t"
    "ds_read_b64_tr_b16 v[216:217], v228 offset:9728\n\t"
    "v_mfma_f32_32x32x16_bf16 v[96:111], a[228:231], a[132:135], v[96:111]\n\t"
    "v_add_f32_e32 v235, v235, v55\n\t"
    "v_exp_f32_e32 v41, v41\n\t"
    "v_add_f32_e32 v232, v232, v40\n\t"
    "ds_read_b64_tr_b16 v[218:219], v228 offset:11776\n\t"
    "v_mfma_f32_32x32x16_bf16 v[112:127], a[228:231], a[164:167], v[112:127]\n\t"
    "v_exp_f32_e32 v42, v42\n\t"
    "v_add_f32_e32 v233, v233, v41\n\t"
    "v_exp_f32_e32 v43, v43\n\t"
    "ds_read_b64_tr_b16 v[172:173], v228 offset:12288\n\t"
    "v_mfma_f32_32x32x16_bf16 v[96:111], a[232:235], a[136:139], v[96:111]\n\t"
    "v_add_f32_e32 v232, v232, v42\n\t"
    "v_exp_f32_e32 v44, v44\n\t"
    "v_add_f32_e32 v233, v233, v43\n\t"
    "v_exp_f32_e32 v45, v45\n\t"
    "ds_read_b64_tr_b16 v[174:175], v228 offset:14336\n\t"
    "v_mfma_f32_32x32x16_bf16 v[112:127], a[232:235], a[168:171], v[112:127]\n\t"
    "v_add_f32_e32 v232, v232, v44\n\t"
    "v_exp_f32_e32 v46, v46\n\t"
    "v_add_f32_e32 v233, v233, v45\n\t"
    "ds_read_b64_tr_b16 v[188:189], v228 offset:12800\n\t"
    "ds_read_b64_tr_b16 v[190:191], v228 offset:14848\n\t"
    "v_mfma_f32_32x32x16_bf16 v[96:111], a[236:239], a[140:143], v[96:111]\n\t"
    "v_exp_f32_e32 v47, v47\n\t"
    "v_cvt_pk_bf16_f32 v40, v40, v41\n\t"
    "v_cvt_pk_bf16_f32 v41, v42, v43\n\t"
    "ds_read_b64_tr_b16 v[204:205], v228 offset:13312\n\t"
    "v_mfma_f32_32x32x16_bf16 v[112:127], a[236:239], a[172:175], v[112:127]\n\t"
    "v_cvt_pk_bf16_f32 v42, v44, v45\n\t"
    "v_cvt_pk_bf16_f32 v43, v46, v47\n\t"
    "v_add_f32_e32 v232, v232, v46\n\t"
    "ds_read_b64_tr_b16 v[206:207], v228 offset:15360\n\t"
    "v_mfma_f32_32x32x16_bf16 v[96:111], a[240:243], a[144:147], v[96:111]\n\t"
    "v_exp_f32_e32 v56, v56\n\t"
    "v_add_f32_e32 v233, v233, v47\n\t"
    "v_exp_f32_e32 v57, v57\n\t"
    "ds_read_b64_tr_b16 v[220:221], v228 offset:13824\n\t"
    "v_mfma_f32_32x32x16_bf16 v[112:127], a[240:243], a[176:179], v[112:127]\n\t"
    "v_add_f32_e32 v234, v234, v56\n\t"
    "v_exp_f32_e32 v58, v58\n\t"
    "v_add_f32_e32 v235, v235, v57\n\t"
    "ds_read_b64_tr_b16 v[222:223], v228 offset:15872\n\t"
    "v_mfma_f32_32x32x16_bf16 v[96:111], a[244:247], a[148:151], v[96:111]\n\t"
    "v_exp_f32_e32 v59, v59\n\t"
    "v_add_f32_e32 v234, v234, v58\n\t"
    "v_exp_f32_e32 v60, v60\n\t"
    "v_mfma_f32_32x32x16_bf16 v[112:127], a[244:247], a[180:183], v[112:127]\n\t"
    "v_add_f32_e32 v235, v235, v59\n\t"
    "v_exp_f32_e32 v61, v61\n\t"
    "v_add_f32_e32 v234, v234, v60\n\t"
    "v_mfma_f32_32x32x16_bf16 v[96:111], a[248:251], a[152:155], v[96:111]\n\t"
    "v_exp_f32_e32 v62, v62\n\t"
    "v_add_f32_e32 v235, v235, v61\n\t"
    "v_exp_f32_e32 v63, v63\n\t"
    "v_mfma_f32_32x32x16_bf16 v[112:127], a[248:251], a[184:187], v[112:127]\n\t"
    "v_cvt_pk_bf16_f32 v56, v56, v57\n\t"
    "v_cvt_pk_bf16_f32 v57, v58, v59\n\t"
    "v_cvt_pk_bf16_f32 v58, v60, v61\n\t"
    "v_mfma_f32_32x32x16_bf16 v[96:111], a[252:255], a[156:159], v[96:111]\n\t"
    "v_cvt_pk_bf16_f32 v59, v62, v63\n\t"
    "v_add_f32_e32 v234, v234, v62\n\t"
    "v_add_f32_e32 v235, v235, v63\n\t"
    "v_mfma_f32_32x32x16_bf16 v[112:127], a[252:255], a[188:191], v[112:127]\n\t"
    "v_max_f32_e32 v242, v232, v233\n\t"
    "v_max3_f32 v242, v242, v234, v235\n\t"
    "v_cmp_lt_f32_e32 vcc, s54, v242\n\t"
    "s_getpc_b64 s[56:57]\n\t"
    "s_cbranch_vccnz RARE_0_%=\n\t"
    "RET_a1l_%=:\n\t"
    "v_add_f32_e32 v236, v236, v232\n\t"
    "v_add_f32_e32 v237, v237, v233\n\t"
    "v_add_f32_e32 v238, v238, v234\n\t"
    "v_add_f32_e32 v239, v239, v235\n\t"
    "s_waitcnt vmcnt(0) lgkmcnt(0)\n\t"
    "s_barrier\n\t"
    "v_mfma_f32_32x32x16_bf16 a[0:15], v[160:163], v[0:3], a[0:15]\n\t"
    "v_mfma_f32_32x32x16_bf16 a[16:31], v[160:163], v[16:19], a[16:31]\n\t"
    "v_mfma_f32_32x32x16_bf16 a[32:47], v[176:179], v[0:3], a[32:47]\n\t"
    "v_mfma_f32_32x32x16_bf16 a[48:63], v[176:179], v[16:19], a[48:63]\n\t"
    "v_mfma_f32_32x32x16_bf16 a[64:79], v[192:195], v[0:3], a[64:79]\n\t"
    "v_mfma_f32_32x32x16_bf16 a[80:95], v[192:195], v[16:19], a[80:95]\n\t"
    "v_mfma_f32_32x32x16_bf16 a[96:111], v[208:211], v[0:3], a[96:111]\n\t"
    "v_mfma_f32_32x32x16_bf16 a[112:127], v[208:211], v[16:19], a[112:127]\n\t"
    "v_mfma_f32_32x32x16_bf16 a[0:15], v[164:167], v[8:11], a[0:15]\n\t"
    "s_add_i32 s50, s50, 16384\n\t"
    "v_mfma_f32_32x32x16_bf16 a[16:31], v[164:167], v[24:27], a[16:31]\n\t"
    "v_exp_f32_e32 v64, v64\n\t"
    "v_exp_f32_e32 v65, v65\n\t"
    "v_exp_f32_e32 v66, v66\n\t"
    "v_mfma_f32_32x32x16_bf16 a[32:47], v[180:183], v[8:11], a[32:47]\n\t"
    "v_exp_f32_e32 v67, v67\n\t"
    "v_add_f32_e32 v232, v64, v66\n\t"
    "v_exp_f32_e32 v68, v68\n\t"
    "v_mfma_f32_32x32x16_bf16 a[48:63], v[180:183], v[24:27], a[48:63]\n\t"
    "v_add_f32_e32 v233, v65, v67\n\t"
    "v_exp_f32_e32 v69, v69\n\t"
    "v_mfma_f32_32x32x16_bf16 a[64:79], v[196:199], v[8:11], a[64:79]\n\t"
    "v_add_f32_e32 v232, v232, v68\n\t"
    "v_exp_f32_e32 v70, v70\n\t"
    "v_add_f32_e32 v233, v233, v69\n\t"
    "v_mfma_f32_32x32x16_bf16 a[80:95], v[196:199], v[24:27], a[80:95]\n\t"
    "v_exp_f32_e32 v71, v71\n\t"
    "v_add_f32_e32 v232, v232, v70\n\t"
    "v_exp_f32_e32 v80, v80\n\t"
    "v_mfma_f32_32x32x16_bf16 a[96:111], v[212:215], v[8:11], a[96:111]\n\t"
    "v_add_f32_e32 v233, v233, v71\n\t"
    "v_exp_f32_e32 v81, v81\n\t"
    "v_mfma_f32_32x32x16_bf16 a[112:127], v[212:215], v[24:27], a[112:127]\n\t"
    "v_exp_f32_e32 v82, v82\n\t"
    "v_exp_f32_e32 v83, v83\n\t"
    "v_add_f32_e32 v234, v80, v82\n\t"
    "v_mfma_f32_32x32x16_bf16 a[0:15], v[168:171], v[32:35], a[0:15]\n\t"
    "v_exp_f32_e32 v84, v84\n\t"
    "v_add_f32_e32 v235, v81, v83\n\t"
    "v_mfma_f32_32x32x16_bf16 a[16:31], v[168:171], v[48:51], a[16:31]\n\t"
    "v_exp_f32_e32 v85, v85\n\t"
    "v_add_f32_e32 v234, v234, v84\n\t"
    "v_exp_f32_e32 v86, v86\n\t"
    "v_mfma_f32_32x32x16_bf16 a[32:47], v[184:187], v[32:35], a[32:47]\n\t"
    "v_add_f32_e32 v235, v235, v85\n\t"
    "v_exp_f32_e32 v87, v87\n\t"
    "v_add_f32_e32 v234, v234, v86\n\t"
    "v_mfma_f32_32x32x16_bf16 a[48:63], v[184:187], v[48:51], a[48:63]\n\t"
    "v_exp_f32_e32 v72, v72\n\t"
    "v_add_f32_e32 v235, v235, v87\n\t"
    "v_mfma_f32_32x32x16_bf16 a[64:79], v[200:203], v[32:35], a[64:79]\n\t"
    "v_exp_f32_e32 v73, v73\n\t"
    "v_add_f32_e32 v232, v232, v72\n\t"
    "v_exp_f32_e32 v74, v74\n\t"
    "v_mfma_f32_32x32x16_bf16 a[80:95], v[200:203], v[48:51], a[80:95]\n\t"
    "v_add_f32_e32 v233, v233, v73\n\t"
    "v_exp_f32_e32 v75, v75\n\t"
    "v_mfma_f32_32x32x16_bf16 a[96:111], v[216:219], v[32:35], a[96:111]\n\t"
    "v_add_f32_e32 v232, v232, v74\n\t"
    "v_exp_f32_e32 v76, v76\n\t"
    "v_add_f32_e32 v233, v233, v75\n\t"
    "v_mfma_f32_32x32x16_bf16 a[112:127], v[216:219], v[48:51], a[112:127]\n\t"
    "v_exp_f32_e32 v77, v77\n\t"
    "v_add_f32_e32 v232, v232, v76\n\t"
    "v_exp_f32_e32 v78, v78\n\t"
    "v_mfma_f32_32x32x16_bf16 a[0:15], v[172:175], v[40:43], a[0:15]\n\t"
    "v_add_f32_e32 v233, v233, v77\n\t"
    "v_exp_f32_e32 v79, v79\n\t"
    "v_mfma_f32_32x32x16_bf16 a[16:31], v[172:175], v[56:59], a[16:31]\n\t"
    "v_add_f32_e32 v232, v232, v78\n\t"
    "v_exp_f32_e32 v88, v88\n\t"
    "v_add_f32_e32 v233, v233, v79\n\t"
    "v_mfma_f32_32x32x16_bf16 a[32:47], v[188:191], v[40:43], a[32:47]\n\t"
    "v_exp_f32_e32 v89, v89\n\t"
    "v_add_f32_e32 v234, v234, v88\n\t"
    "v_mfma_f32_32x32x16_bf16 a[48:63], v[188:191], v[56:59], a[48:63]\n\t"
    "v_exp_f32_e32 v90, v90\n\t"
    "v_add_f32_e32 v235, v235, v89\n\t"
    "v_exp_f32_e32 v91, v91\n\t"
    "v_mfma_f32_32x32x16_bf16 a[64:79], v[204:207], v[40:43], a[64:79]\n\t"
    "v_add_f32_e32 v234, v234, v90\n\t"
    "v_exp_f32_e32 v92, v92\n\t"
    "v_add_f32_e32 v235, v235, v91\n\t"
    "v_mfma_f32_32x32x16_bf16 a[80:95], v[204:207], v[56:59], a[80:95]\n\t"
    "v_exp_f32_e32 v93, v93\n\t"
    "v_add_f32_e32 v234, v234, v92\n\t"
    "v_mfma_f32_32x32x16_bf16 a[96:111], v[220:223], v[40:43], a[96:111]\n\t"
    "v_exp_f32_e32 v94, v94\n\t"
    "v_add_f32_e32 v235, v235, v93\n\t"
    "v_exp_f32_e32 v95, v95\n\t"
    "v_mfma_f32_32x32x16_bf16 a[112:127], v[220:223], v[56:59], a[112:127]\n\t"
    "v_add_f32_e32 v234, v234, v94\n\t"
    "v_add_f32_e32 v235, v235, v95\n\t"
    "s_waitcnt lgkmcnt(0)\n\t"
    "ds_read_b64_tr_b16 v[160:161], v228 offset:16384\n\t"
    "ds_read_b64_tr_b16 v[162:163], v228 offset:18432\n\t"
    "ds_read_b64_tr_b16 v[176:177], v228 offset:16896\n\t"
    "ds_read_b64_tr_b16 v[178:179], v228 offset:18944\n\t"
    "ds_read_b64_tr_b16 v[192:193], v228 offset:17408\n\t"
    "ds_read_b64_tr_b16 v[194:195], v228 offset:19456\n\t"
    "ds_read_b64_tr_b16 v[208:209], v228 offset:17920\n\t"
    "ds_read_b64_tr_b16 v[210:211], v228 offset:19968\n\t"
    "ds_read_b64_tr_b16 v[164:165], v228 offset:20480\n\t"
    "ds_read_b64_tr_b16 v[166:167], v228 offset:22528\n\t"
    "ds_read_b64_tr_b16 v[180:181], v228 offset:20992\n\t"
    "ds_read_b64_tr_b16 v[182:183], v228 offset:23040\n\t"
    "ds_read_b64_tr_b16 v[196:197], v228 offset:21504\n\t"
    "ds_read_b64_tr_b16 v[198:199], v228 offset:23552\n\t"
    "ds_read_b64_tr_b16 v[212:213], v228 offset:22016\n\t"
    "ds_read_b64_tr_b16 v[214:215], v228 offset:24064\n\t"
    "ds_read_b64_tr_b16 v[168:169], v228 offset:24576\n\t"
    "ds_read_b64_tr_b16 v[170:171], v228 offset:26624\n\t"
    "ds_read_b64_tr_b16 v[184:185], v228 offset:25088\n\t"
    "ds_read_b64_tr_b16 v[186:187], v228 offset:27136\n\t"
    "ds_read_b64_tr_b16 v[200:201], v228 offset:25600\n\t"
    "ds_read_b64_tr_b16 v[202:203], v228 offset:27648\n\t"
    "ds_read_b64_tr_b16 v[216:217], v228 offset:26112\n\t"
    "ds_read_b64_tr_b16 v[218:219], v228 offset:28160\n\t"
    "ds_read_b64_tr_b16 v[172:173], v228 offset:28672\n\t"
    "ds_read_b64_tr_b16 v[174:175], v228 offset:30720\n\t"
    "ds_read_b64_tr_b16 v[188:189], v228 offset:29184\n\t"
    "ds_read_b64_tr_b16 v[190:191], v228 offset:31232\n\t"
    "ds_read_b64_tr_b16 v[204:205], v228 offset:29696\n\t"
    "ds_read_b64_tr_b16 v[206:207], v228 offset:31744\n\t"
    "ds_read_b64_tr_b16 v[220:221], v228 offset:30208\n\t"
    "ds_read_b64_tr_b16 v[222:223], v228 offset:32256\n\t"
    "v_exp_f32_e32 v96, v96\n\t"
    "s_nop 0\n\t"
    "v_add_f32_e32 v232, v232, v96\n\t"
    "v_exp_f32_e32 v97, v97\n\t"
    "s_nop 0\n\t"
    "v_add_f32_e32 v233, v233, v97\n\t"
    "v_exp_f32_e32 v98, v98\n\t"
    "s_nop 0\n\t"
    "v_add_f32_e32 v232, v232, v98\n\t"
    "v_exp_f32_e32 v99, v99\n\t"
    "s_nop 0\n\t"
    "v_add_f32_e32 v233, v233, v99\n\t"
    "v_exp_f32_e32 v100, v100\n\t"
    "s_nop 0\n\t"
    "v_add_f32_e32 v232, v232, v100\n\t"
    "v_exp_f32_e32 v101, v101\n\t"
    "s_nop 0\n\t"
    "v_add_f32_e32 v233, v233, v101\n\t"
    "v_exp_f32_e32 v102, v102\n\t"
    "s_nop 0\n\t"
    "v_add_f32_e32 v232, v232, v102\n\t"
    "v_exp_f32_e32 v103, v103\n\t"
    "s_nop 0\n\t"
    "v_add_f32_e32 v233, v233, v103\n\t"
    "v_exp_f32_e32 v112, v112\n\t"
    "s_nop 0\n\t"
    "v_add_f32_e32 v234, v234, v112\n\t"
    "v_exp_f32_e32 v113, v113\n\t"
    "s_nop 0\n\t"
    "v_add_f32_e32 v235, v235, v113\n\t"
    "v_exp_f32_e32 v114, v114\n\t"
    "s_nop 0\n\t"
    "v_add_f32_e32 v234, v234, v114\n\t"
    "v_exp_f32_e32 v115, v115\n\t"
    "s_nop 0\n\t"
    "v_add_f32_e32 v235, v235, v115\n\t"
    "v_exp_f32_e32 v116, v116\n\t"
    "s_nop 0\n\t"
    "v_add_f32_e32 v234, v234, v116\n\t"
    "v_exp_f32_e32 v117, v117\n\t"
    "s_nop 0\n\t"
    "v_add_f32_e32 v235, v235, v117\n\t"
    "v_exp_f32_e32 v118, v118\n\t"
    "s_nop 0\n\t"
    "v_add_f32_e32 v234, v234, v118\n\t"
    "v_exp_f32_e32 v119, v119\n\t"
    "s_nop 0\n\t"
    "v_add_f32_e32 v235, v235, v119\n\t"
    "v_exp_f32_e32 v104, v104\n\t"
    "s_nop 0\n\t"
    "v_add_f32_e32 v232, v232, v104\n\t"
    "v_exp_f32_e32 v105, v105\n\t"
    "s_nop 0\n\t"
    "v_add_f32_e32 v233, v233, v105\n\t"
    "v_exp_f32_e32 v106, v106\n\t"
    "s_nop 0\n\t"
    "v_add_f32_e32 v232, v232, v106\n\t"
    "v_exp_f32_e32 v107, v107\n\t"
    "s_nop 0\n\t"
    "v_add_f32_e32 v233, v233, v107\n\t"
    "v_exp_f32_e32 v108, v108\n\t"
    "s_nop 0\n\t"
    "v_add_f32_e32 v232, v232, v108\n\t"
    "v_exp_f32_e32 v109, v109\n\t"
    "s_nop 0\n\t"
    "v_add_f32_e32 v233, v233, v109\n\t"
    "v_exp_f32_e32 v110, v110\n\t"
    "s_nop 0\n\t"
    "v_add_f32_e32 v232, v232, v110\n\t"
    "v_exp_f32_e32 v111, v111\n\t"
    "s_nop 0\n\t"
    "v_add_f32_e32 v233, v233, v111\n\t"
    "v_exp_f32_e32 v120, v120\n\t"
    "s_nop 0\n\t"
    "v_add_f32_e32 v234, v234, v120\n\t"
    "v_exp_f32_e32 v121, v121\n\t"
    "s_nop 0\n\t"
    "v_add_f32_e32 v235, v235, v121\n\t"
    "v_exp_f32_e32 v122, v122\n\t"
    "s_nop 0\n\t"
    "v_add_f32_e32 v234, v234, v122\n\t"
    "v_exp_f32_e32 v123, v123\n\t"
    "s_nop 0\n\t"
    "v_add_f32_e32 v235, v235, v123\n\t"
    "v_exp_f32_e32 v124, v124\n\t"
    "s_nop 0\n\t"
    "v_add_f32_e32 v234, v234, v124\n\t"
    "v_exp_f32_e32 v125, v125\n\t"
    "s_nop 0\n\t"
    "v_add_f32_e32 v235, v235, v125\n\t"
    "v_exp_f32_e32 v126, v126\n\t"
    "s_nop 0\n\t"
    "v_add_f32_e32 v234, v234, v126\n\t"
    "v_exp_f32_e32 v127, v127\n\t"
    "s_nop 0\n\t"
    "v_add_f32_e32 v235, v235, v127\n\t"
    "v_cvt_pk_bf16_f32 v64, v64, v65\n\t"
    "v_cvt_pk_bf16_f32 v65, v66, v67\n\t"
    "v_cvt_pk_bf16_f32 v66, v68, v69\n\t"
    "v_cvt_pk_bf16_f32 v67, v70, v71\n\t"
    "v_cvt_pk_bf16_f32 v80, v80, v81\n\t"
    "v_cvt_pk_bf16_f32 v81, v82, v83\n\t"
    "v_cvt_pk_bf16_f32 v82, v84, v85\n\t"
    "v_cvt_pk_bf16_f32 v83, v86, v87\n\t"
    "v_cvt_pk_bf16_f32 v72, v72, v73\n\t"
    "v_cvt_pk_bf16_f32 v73, v74, v75\n\t"
    "v_cvt_pk_bf16_f32 v74, v76, v77\n\t"
    "v_cvt_pk_bf16_f32 v75, v78, v79\n\t"
    "v_cvt_pk_bf16_f32 v88, v88, v89\n\t"
    "v_cvt_pk_bf16_f32 v89, v90, v91\n\t"
    "v_cvt_pk_bf16_f32 v90, v92, v93\n\t"
    "v_cvt_pk_bf16_f32 v91, v94, v95\n\t"
    "v_cvt_pk_bf16_f32 v96, v96, v97\n\t"
    "v_cvt_pk_bf16_f32 v97, v98, v99\n\t"
    "v_cvt_pk_bf16_f32 v98, v100, v101\n\t"
    "v_cvt_pk_bf16_f32 v99, v102, v103\n\t"
    "v_cvt_pk_bf16_f32 v112, v112, v113\n\t"
    "v_cvt_pk_bf16_f32 v113, v114, v115\n\t"
    "v_cvt_pk_bf16_f32 v114, v116, v117\n\t"
    "v_cvt_pk_bf16_f32 v115, v118, v119\n\t"
    "v_cvt_pk_bf16_f32 v104, v104, v105\n\t"
    "v_cvt_pk_bf16_f32 v105, v106, v107\n\t"
    "v_cvt_pk_bf16_f32 v106, v108, v109\n\t"
    "v_cvt_pk_bf16_f32 v107, v110, v111\n\t"
    "v_cvt_pk_bf16_f32 v120, v120, v121\n\t"
    "v_cvt_pk_bf16_f32 v121, v122, v123\n\t"
    "v_cvt_pk_bf16_f32 v122, v124, v125\n\t"
    "v_cvt_pk_bf16_f32 v123, v126, v127\n\t"
    "v_max_f32_e32 v242, v232, v233\n\t"
    "v_max3_f32 v242, v242, v234, v235\n\t"
    "v_cmp_lt_f32_e32 vcc, s54, v242\n\t"
    "s_getpc_b64 s[56:57]\n\t"
    "s_cbranch_vccnz RARE_1_%=\n\t"
    "v_add_f32_e32 v236, v236, v232\n\t"
    "v_add_f32_e32 v237, v237, v233\n\t"
    "v_add_f32_e32 v238, v238, v234\n\t"
    "v_add_f32_e32 v239, v239, v235\n\t"
    "s_waitcnt lgkmcnt(0)\n\t"
    "v_mfma_f32_32x32x16_bf16 a[0:15], v[160:163], v[64:67], a[0:15]\n\t"
    "v_mfma_f32_32x32x16_bf16 a[16:31], v[160:163], v[80:83], a[16:31]\n\t"
    "v_mfma_f32_32x32x16_bf16 a[32:47], v[176:179], v[64:67], a[32:47]\n\t"
    "v_mfma_f32_32x32x16_bf16 a[48:63], v[176:179], v[80:83], a[48:63]\n\t"
    "v_mfma_f32_32x32x16_bf16 a[64:79], v[192:195], v[64:67], a[64:79]\n\t"
    "v_mfma_f32_32x32x16_bf16 a[80:95], v[192:195], v[80:83], a[80:95]\n\t"
    "v_mfma_f32_32x32x16_bf16 a[96:111], v[208:211], v[64:67], a[96:111]\n\t"
    "v_mfma_f32_32x32x16_bf16 a[112:127], v[208:211], v[80:83], a[112:127]\n\t"
    "v_mfma_f32_32x32x16_bf16 a[0:15], v[164:167], v[72:75], a[0:15]\n\t"
    "v_mfma_f32_32x32x16_bf16 a[16:31], v[164:167], v[88:91], a[16:31]\n\t"
    "v_mfma_f32_32x32x16_bf16 a[32:47], v[180:183], v[72:75], a[32:47]\n\t"
    "v_mfma_f32_32x32x16_bf16 a[48:63], v[180:183], v[88:91], a[48:63]\n\t"
    "v_mfma_f32_32x32x16_bf16 a[64:79], v[196:199], v[72:75], a[64:79]\n\t"
    "v_mfma_f32_32x32x16_bf16 a[80:95], v[196:199], v[88:91], a[80:95]\n\t"
    "v_mfma_f32_32x32x16_bf16 a[96:111], v[212:215], v[72:75], a[96:111]\n\t"
    "v_mfma_f32_32x32x16_bf16 a[112:127], v[212:215], v[88:91], a[112:127]\n\t"
    "v_mfma_f32_32x32x16_bf16 a[0:15], v[168:171], v[96:99], a[0:15]\n\t"
    "v_mfma_f32_32x32x16_bf16 a[16:31], v[168:171], v[112:115], a[16:31]\n\t"
    "v_mfma_f32_32x32x16_bf16 a[32:47], v[184:187], v[96:99], a[32:47]\n\t"
    "v_mfma_f32_32x32x16_bf16 a[48:63], v[184:187], v[112:115], a[48:63]\n\t"
    "v_mfma_f32_32x32x16_bf16 a[64:79], v[200:203], v[96:99], a[64:79]\n\t"
    "v_mfma_f32_32x32x16_bf16 a[80:95], v[200:203], v[112:115], a[80:95]\n\t"
    "v_mfma_f32_32x32x16_bf16 a[96:111], v[216:219], v[96:99], a[96:111]\n\t"
    "v_mfma_f32_32x32x16_bf16 a[112:127], v[216:219], v[112:115], a[112:127]\n\t"
    "v_mfma_f32_32x32x16_bf16 a[0:15], v[172:175], v[104:107], a[0:15]\n\t"
    "v_mfma_f32_32x32x16_bf16 a[16:31], v[172:175], v[120:123], a[16:31]\n\t"
    "v_mfma_f32_32x32x16_bf16 a[32:47], v[188:191], v[104:107], a[32:47]\n\t"
    "v_mfma_f32_32x32x16_bf16 a[48:63], v[188:191], v[120:123], a[48:63]\n\t"
    "v_mfma_f32_32x32x16_bf16 a[64:79], v[204:207], v[104:107], a[64:79]\n\t"
    "v_mfma_f32_32x32x16_bf16 a[80:95], v[204:207], v[120:123], a[80:95]\n\t"
    "v_mfma_f32_32x32x16_bf16 a[96:111], v[220:223], v[104:107], a[96:111]\n\t"
    "v_mfma_f32_32x32x16_bf16 a[112:127], v[220:223], v[120:123], a[112:127]\n\t"
    "s_nop 7\n\t"
    "s_nop 7\n\t"
    "EPI_%=:\n\t"
    "v_add_f32_e32 v242, v236, v237\n\t"
    "v_mov_b32_e32 v243, v242\n\t"
    "s_nop 1\n\t"
    "v_permlane32_swap_b32_e32 v242, v243\n\t"
    "v_add_f32_e32 v242, v242, v243\n\t"
    "v_rcp_f32_e32 v244, v242\n\t"
    "v_add_f32_e32 v242, v238, v239\n\t"
    "v_mov_b32_e32 v243, v242\n\t"
    "s_nop 1\n\t"
    "v_permlane32_swap_b32_e32 v242, v243\n\t"
    "v_add_f32_e32 v242, v242, v243\n\t"
    "v_rcp_f32_e32 v245, v242\n\t"
    "v_and_b32_e32 v7, 15, v249\n\t"
    "v_lshlrev_b32_e32 v210, 4, v7\n\t"
    "v_and_b32_e32 v6, 31, v249\n\t"
    "v_lshlrev_b32_e32 v211, 8, v6\n\t"
    "v_lshrrev_b32_e32 v6, 5, v249\n\t"
    "v_lshl_add_u32 v211, v6, 3, v211\n\t"
    "v_add_u32_e32 v211, %[ostg], v211\n\t"
    "v_lshrrev_b32_e32 v6, 4, v249\n\t"
    "v_xor_b32_e32 v7, v7, v6\n\t"
    "v_lshlrev_b32_e32 v6, 8, v6\n\t"
    "v_lshl_add_u32 v6, v7, 4, v6\n\t"
    "v_add_u32_e32 v212, %[ostg], v6\n\t"
    "v_xor_b32_e32 v213, 64, v212\n\t"
    "v_xor_b32_e32 v214, 128, v212\n\t"
    "v_xor_b32_e32 v215, 192, v212\n\t"
    "v_lshlrev_b32_e32 v216, 4, v249\n\t"
    "v_accvgpr_read_b32 v0, a0\n\t"
    "v_accvgpr_read_b32 v1, a1\n\t"
    "v_accvgpr_read_b32 v2, a2\n\t"
    "v_accvgpr_read_b32 v3, a3\n\t"
    "v_mul_f32_e32 v0, v0, v244\n\t"
    "v_mul_f32_e32 v1, v1, v244\n\t"
    "v_mul_f32_e32 v2, v2, v244\n\t"
    "v_mul_f32_e32 v3, v3, v244\n\t"
    "v_cvt_pk_bf16_f32 v4, v0, v1\n\t"
    "v_cvt_pk_bf16_f32 v5, v2, v3\n\t"
    "v_xor_b32_e32 v6, 0, v210\n\t"
    "v_add_u32_e32 v6, v6, v211\n\t"
    "ds_write_b64 v6, v[4:5] offset:0\n\t"
    "v_accvgpr_read_b32 v0, a4\n\t"
    "v_accvgpr_read_b32 v1, a5\n\t"
    "v_accvgpr_read_b32 v2, a6\n\t"
    "v_accvgpr_read_b32 v3, a7\n\t"
    "v_mul_f32_e32 v0, v0, v244\n\t"
    "v_mul_f32_e32 v1, v1, v244\n\t"
    "v_mul_f32_e32 v2, v2, v244\n\t"
    "v_mul_f32_e32 v3, v3, v244\n\t"
    "v_cvt_pk_bf16_f32 v4, v0, v1\n\t"
    "v_cvt_pk_bf16_f32 v5, v2, v3\n\t"
    "v_xor_b32_e32 v6, 16, v210\n\t"
    "v_add_u32_e32 v6, v6, v211\n\t"
    "ds_write_b64 v6, v[4:5] offset:0\n\t"
    "v_accvgpr_read_b32 v0, a8\n\t"
    "v_accvgpr_read_b32 v1, a9\n\t"
    "v_accvgpr_read_b32 v2, a10\n\t"
    "v_accvgpr_read_b32 v3, a11\n\t"
    "v_mul_f32_e32 v0, v0, v244\n\t"
    "v_mul_f32_e32 v1, v1, v244\n\t"
    "v_mul_f32_e32 v2, v2, v244\n\t"
    "v_mul_f32_e32 v3, v3, v244\n\t"
    "v_cvt_pk_bf16_f32 v4, v0, v1\n\t"
    "v_cvt_pk_bf16_f32 v5, v2, v3\n\t"
    "v_xor_b32_e32 v6, 32, v210\n\t"
    "v_add_u32_e32 v6, v6, v211\n\t"
    "ds_write_b64 v6, v[4:5] offset:0\n\t"
    "v_accvgpr_read_b32 v0, a12\n\t"
    "v_accvgpr_read_b32 v1, a13\n\t"
    "v_accvgpr_read_b32 v2, a14\n\t"
    "v_accvgpr_read_b32 v3, a15\n\t"
    "v_mul_f32_e32 v0, v0, v244\n\t"
    "v_mul_f32_e32 v1, v1, v244\n\t"
    "v_mul_f32_e32 v2, v2, v244\n\t"
    "v_mul_f32_e32 v3, v3, v244\n\t"
    "v_cvt_pk_bf16_f32 v4, v0, v1\n\t"
    "v_cvt_pk_bf16_f32 v5, v2, v3\n\t"
    "v_xor_b32_e32 v6, 48, v210\n\t"
    "v_add_u32_e32 v6, v6, v211\n\t"
    "ds_write_b64 v6, v[4:5] offset:0\n\t"
    "v_accvgpr_read_b32 v0, a32\n\t"
    "v_accvgpr_read_b32 v1, a33\n\t"
    "v_accvgpr_read_b32 v2, a34\n\t"
    "v_accvgpr_read_b32 v3, a35\n\t"
    "v_mul_f32_e32 v0, v0, v244\n\t"
    "v_mul_f32_e32 v1, v1, v244\n\t"
    "v_mul_f32_e32 v2, v2, v244\n\t"
    "v_mul_f32_e32 v3, v3, v244\n\t"
    "v_cvt_pk_bf16_f32 v4, v0, v1\n\t"
    "v_cvt_pk_bf16_f32 v5, v2, v3\n\t"
    "v_xor_b32_e32 v6, 64, v210\n\t"
    "v_add_u32_e32 v6, v6, v211\n\t"
    "ds_write_b64 v6, v[4:5] offset:0\n\t"
    "v_accvgpr_read_b32 v0, a36\n\t"
    "v_accvgpr_read_b32 v1, a37\n\t"
    "v_accvgpr_read_b32 v2, a38\n\t"
    "v_accvgpr_read_b32 v3, a39\n\t"
    "v_mul_f32_e32 v0, v0, v244\n\t"
    "v_mul_f32_e32 v1, v1, v244\n\t"
    "v_mul_f32_e32 v2, v2, v244\n\t"
    "v_mul_f32_e32 v3, v3, v244\n\t"
    "v_cvt_pk_bf16_f32 v4, v0, v1\n\t"
    "v_cvt_pk_bf16_f32 v5, v2, v3\n\t"
    "v_xor_b32_e32 v6, 80, v210\n\t"
    "v_add_u32_e32 v6, v6, v211\n\t"
    "ds_write_b64 v6, v[4:5] offset:0\n\t"
    "v_accvgpr_read_b32 v0, a40\n\t"
    "v_accvgpr_read_b32 v1, a41\n\t"
    "v_accvgpr_read_b32 v2, a42\n\t"
    "v_accvgpr_read_b32 v3, a43\n\t"
    "v_mul_f32_e32 v0, v0, v244\n\t"
    "v_mul_f32_e32 v1, v1, v244\n\t"
    "v_mul_f32_e32 v2, v2, v244\n\t"
    "v_mul_f32_e32 v3, v3, v244\n\t"
    "v_cvt_pk_bf16_f32 v4, v0, v1\n\t"
    "v_cvt_pk_bf16_f32 v5, v2, v3\n\t"
    "v_xor_b32_e32 v6, 96, v210\n\t"
    "v_add_u32_e32 v6, v6, v211\n\t"
    "ds_write_b64 v6, v[4:5] offset:0\n\t"
    "v_accvgpr_read_b32 v0, a44\n\t"
    "v_accvgpr_read_b32 v1, a45\n\t"
    "v_accvgpr_read_b32 v2, a46\n\t"
    "v_accvgpr_read_b32 v3, a47\n\t"
    "v_mul_f32_e32 v0, v0, v244\n\t"
    "v_mul_f32_e32 v1, v1, v244\n\t"
    "v_mul_f32_e32 v2, v2, v244\n\t"
    "v_mul_f32_e32 v3, v3, v244\n\t"
    "v_cvt_pk_bf16_f32 v4, v0, v1\n\t"
    "v_cvt_pk_bf16_f32 v5, v2, v3\n\t"
    "v_xor_b32_e32 v6, 112, v210\n\t"
    "v_add_u32_e32 v6, v6, v211\n\t"
    "ds_write_b64 v6, v[4:5] offset:0\n\t"
    "v_accvgpr_read_b32 v0, a64\n\t"
    "v_accvgpr_read_b32 v1, a65\n\t"
    "v_accvgpr_read_b32 v2, a66\n\t"
    "v_accvgpr_read_b32 v3, a67\n\t"
    "v_mul_f32_e32 v0, v0, v244\n\t"
    "v_mul_f32_e32 v1, v1, v244\n\t"
    "v_mul_f32_e32 v2, v2, v244\n\t"
    "v_mul_f32_e32 v3, v3, v244\n\t"
    "v_cvt_pk_bf16_f32 v4, v0, v1\n\t"
    "v_cvt_pk_bf16_f32 v5, v2, v3\n\t"
    "v_xor_b32_e32 v6, 128, v210\n\t"
    "v_add_u32_e32 v6, v6, v211\n\t"
    "ds_write_b64 v6, v[4:5] offset:0\n\t"
    "v_accvgpr_read_b32 v0, a68\n\t"
    "v_accvgpr_read_b32 v1, a69\n\t"
    "v_accvgpr_read_b32 v2, a70\n\t"
    "v_accvgpr_read_b32 v3, a71\n\t"
    "v_mul_f32_e32 v0, v0, v244\n\t"
    "v_mul_f32_e32 v1, v1, v244\n\t"
    "v_mul_f32_e32 v2, v2, v244\n\t"
    "v_mul_f32_e32 v3, v3, v244\n\t"
    "v_cvt_pk_bf16_f32 v4, v0, v1\n\t"
    "v_cvt_pk_bf16_f32 v5, v2, v3\n\t"
    "v_xor_b32_e32 v6, 144, v210\n\t"
    "v_add_u32_e32 v6, v6, v211\n\t"
    "ds_write_b64 v6, v[4:5] offset:0\n\t"
    "v_accvgpr_read_b32 v0, a72\n\t"
    "v_accvgpr_read_b32 v1, a73\n\t"
    "v_accvgpr_read_b32 v2, a74\n\t"
    "v_accvgpr_read_b32 v3, a75\n\t"
    "v_mul_f32_e32 v0, v0, v244\n\t"
    "v_mul_f32_e32 v1, v1, v244\n\t"
    "v_mul_f32_e32 v2, v2, v244\n\t"
    "v_mul_f32_e32 v3, v3, v244\n\t"
    "v_cvt_pk_bf16_f32 v4, v0, v1\n\t"
    "v_cvt_pk_bf16_f32 v5, v2, v3\n\t"
    "v_xor_b32_e32 v6, 160, v210\n\t"
    "v_add_u32_e32 v6, v6, v211\n\t"
    "ds_write_b64 v6, v[4:5] offset:0\n\t"
    "v_accvgpr_read_b32 v0, a76\n\t"
    "v_accvgpr_read_b32 v1, a77\n\t"
    "v_accvgpr_read_b32 v2, a78\n\t"
    "v_accvgpr_read_b32 v3, a79\n\t"
    "v_mul_f32_e32 v0, v0, v244\n\t"
    "v_mul_f32_e32 v1, v1, v244\n\t"
    "v_mul_f32_e32 v2, v2, v244\n\t"
    "v_mul_f32_e32 v3, v3, v244\n\t"
    "v_cvt_pk_bf16_f32 v4, v0, v1\n\t"
    "v_cvt_pk_bf16_f32 v5, v2, v3\n\t"
    "v_xor_b32_e32 v6, 176, v210\n\t"
    "v_add_u32_e32 v6, v6, v211\n\t"
    "ds_write_b64 v6, v[4:5] offset:0\n\t"
    "v_accvgpr_read_b32 v0, a96\n\t"
    "v_accvgpr_read_b32 v1, a97\n\t"
    "v_accvgpr_read_b32 v2, a98\n\t"
    "v_accvgpr_read_b32 v3, a99\n\t"
    "v_mul_f32_e32 v0, v0, v244\n\t"
    "v_mul_f32_e32 v1, v1, v244\n\t"
    "v_mul_f32_e32 v2, v2, v244\n\t"
    "v_mul_f32_e32 v3, v3, v244\n\t"
    "v_cvt_pk_bf16_f32 v4, v0, v1\n\t"
    "v_cvt_pk_bf16_f32 v5, v2, v3\n\t"
    "v_xor_b32_e32 v6, 192, v210\n\t"
    "v_add_u32_e32 v6, v6, v211\n\t"
    "ds_write_b64 v6, v[4:5] offset:0\n\t"
    "v_accvgpr_read_b32 v0, a100\n\t"
    "v_accvgpr_read_b32 v1, a101\n\t"
    "v_accvgpr_read_b32 v2, a102\n\t"
    "v_accvgpr_read_b32 v3, a103\n\t"
    "v_mul_f32_e32 v0, v0, v244\n\t"
    "v_mul_f32_e32 v1, v1, v244\n\t"
    "v_mul_f32_e32 v2, v2, v244\n\t"
    "v_mul_f32_e32 v3, v3, v244\n\t"
    "v_cvt_pk_bf16_f32 v4, v0, v1\n\t"
    "v_cvt_pk_bf16_f32 v5, v2, v3\n\t"
    "v_xor_b32_e32 v6, 208, v210\n\t"
    "v_add_u32_e32 v6, v6, v211\n\t"
    "ds_write_b64 v6, v[4:5] offset:0\n\t"
    "v_accvgpr_read_b32 v0, a104\n\t"
    "v_accvgpr_read_b32 v1, a105\n\t"
    "v_accvgpr_read_b32 v2, a106\n\t"
    "v_accvgpr_read_b32 v3, a107\n\t"
    "v_mul_f32_e32 v0, v0, v244\n\t"
    "v_mul_f32_e32 v1, v1, v244\n\t"
    "v_mul_f32_e32 v2, v2, v244\n\t"
    "v_mul_f32_e32 v3, v3, v244\n\t"
    "v_cvt_pk_bf16_f32 v4, v0, v1\n\t"
    "v_cvt_pk_bf16_f32 v5, v2, v3\n\t"
    "v_xor_b32_e32 v6, 224, v210\n\t"
    "v_add_u32_e32 v6, v6, v211\n\t"
    "ds_write_b64 v6, v[4:5] offset:0\n\t"
    "v_accvgpr_read_b32 v0, a108\n\t"
    "v_accvgpr_read_b32 v1, a109\n\t"
    "v_accvgpr_read_b32 v2, a110\n\t"
    "v_accvgpr_read_b32 v3, a111\n\t"
    "v_mul_f32_e32 v0, v0, v244\n\t"
    "v_mul_f32_e32 v1, v1, v244\n\t"
    "v_mul_f32_e32 v2, v2, v244\n\t"
    "v_mul_f32_e32 v3, v3, v244\n\t"
    "v_cvt_pk_bf16_f32 v4, v0, v1\n\t"
    "v_cvt_pk_bf16_f32 v5, v2, v3\n\t"
    "v_xor_b32_e32 v6, 240, v210\n\t"
    "v_add_u32_e32 v6, v6, v211\n\t"
    "ds_write_b64 v6, v[4:5] offset:0\n\t"
    "v_accvgpr_read_b32 v0, a16\n\t"
    "v_accvgpr_read_b32 v1, a17\n\t"
    "v_accvgpr_read_b32 v2, a18\n\t"
    "v_accvgpr_read_b32 v3, a19\n\t"
    "v_mul_f32_e32 v0, v0, v245\n\t"
    "v_mul_f32_e32 v1, v1, v245\n\t"
    "v_mul_f32_e32 v2, v2, v245\n\t"
    "v_mul_f32_e32 v3, v3, v245\n\t"
    "v_cvt_pk_bf16_f32 v4, v0, v1\n\t"
    "v_cvt_pk_bf16_f32 v5, v2, v3\n\t"
    "v_xor_b32_e32 v6, 0, v210\n\t"
    "v_add_u32_e32 v6, v6, v211\n\t"
    "ds_write_b64 v6, v[4:5] offset:8192\n\t"
    "v_accvgpr_read_b32 v0, a20\n\t"
    "v_accvgpr_read_b32 v1, a21\n\t"
    "v_accvgpr_read_b32 v2, a22\n\t"
    "v_accvgpr_read_b32 v3, a23\n\t"
    "v_mul_f32_e32 v0, v0, v245\n\t"
    "v_mul_f32_e32 v1, v1, v245\n\t"
    "v_mul_f32_e32 v2, v2, v245\n\t"
    "v_mul_f32_e32 v3, v3, v245\n\t"
    "v_cvt_pk_bf16_f32 v4, v0, v1\n\t"
    "v_cvt_pk_bf16_f32 v5, v2, v3\n\t"
    "v_xor_b32_e32 v6, 16, v210\n\t"
    "v_add_u32_e32 v6, v6, v211\n\t"
    "ds_write_b64 v6, v[4:5] offset:8192\n\t"
    "v_accvgpr_read_b32 v0, a24\n\t"
    "v_accvgpr_read_b32 v1, a25\n\t"
    "v_accvgpr_read_b32 v2, a26\n\t"
    "v_accvgpr_read_b32 v3, a27\n\t"
    "v_mul_f32_e32 v0, v0, v245\n\t"
    "v_mul_f32_e32 v1, v1, v245\n\t"
    "v_mul_f32_e32 v2, v2, v245\n\t"
    "v_mul_f32_e32 v3, v3, v245\n\t"
    "v_cvt_pk_bf16_f32 v4, v0, v1\n\t"
    "v_cvt_pk_bf16_f32 v5, v2, v3\n\t"
    "v_xor_b32_e32 v6, 32, v210\n\t"
    "v_add_u32_e32 v6, v6, v211\n\t"
    "ds_write_b64 v6, v[4:5] offset:8192\n\t"
    "v_accvgpr_read_b32 v0, a28\n\t"
    "v_accvgpr_read_b32 v1, a29\n\t"
    "v_accvgpr_read_b32 v2, a30\n\t"
    "v_accvgpr_read_b32 v3, a31\n\t"
    "v_mul_f32_e32 v0, v0, v245\n\t"
    "v_mul_f32_e32 v1, v1, v245\n\t"
    "v_mul_f32_e32 v2, v2, v245\n\t"
    "v_mul_f32_e32 v3, v3, v245\n\t"
    "v_cvt_pk_bf16_f32 v4, v0, v1\n\t"
    "v_cvt_pk_bf16_f32 v5, v2, v3\n\t"
    "v_xor_b32_e32 v6, 48, v210\n\t"
    "v_add_u32_e32 v6, v6, v211\n\t"
    "ds_write_b64 v6, v[4:5] offset:8192\n\t"
    "v_accvgpr_read_b32 v0, a48\n\t"
    "v_accvgpr_read_b32 v1, a49\n\t"
    "v_accvgpr_read_b32 v2, a50\n\t"
    "v_accvgpr_read_b32 v3, a51\n\t"
    "v_mul_f32_e32 v0, v0, v245\n\t"
    "v_mul_f32_e32 v1, v1, v245\n\t"
    "v_mul_f32_e32 v2, v2, v245\n\t"
    "v_mul_f32_e32 v3, v3, v245\n\t"
    "v_cvt_pk_bf16_f32 v4, v0, v1\n\t"
    "v_cvt_pk_bf16_f32 v5, v2, v3\n\t"
    "v_xor_b32_e32 v6, 64, v210\n\t"
    "v_add_u32_e32 v6, v6, v211\n\t"
    "ds_write_b64 v6, v[4:5] offset:8192\n\t"
    "v_accvgpr_read_b32 v0, a52\n\t"
    "v_accvgpr_read_b32 v1, a53\n\t"
    "v_accvgpr_read_b32 v2, a54\n\t"
    "v_accvgpr_read_b32 v3, a55\n\t"
    "v_mul_f32_e32 v0, v0, v245\n\t"
    "v_mul_f32_e32 v1, v1, v245\n\t"
    "v_mul_f32_e32 v2, v2, v245\n\t"
    "v_mul_f32_e32 v3, v3, v245\n\t"
    "v_cvt_pk_bf16_f32 v4, v0, v1\n\t"
    "v_cvt_pk_bf16_f32 v5, v2, v3\n\t"
    "v_xor_b32_e32 v6, 80, v210\n\t"
    "v_add_u32_e32 v6, v6, v211\n\t"
    "ds_write_b64 v6, v[4:5] offset:8192\n\t"
    "v_accvgpr_read_b32 v0, a56\n\t"
    "v_accvgpr_read_b32 v1, a57\n\t"
    "v_accvgpr_read_b32 v2, a58\n\t"
    "v_accvgpr_read_b32 v3, a59\n\t"
    "v_mul_f32_e32 v0, v0, v245\n\t"
    "v_mul_f32_e32 v1, v1, v245\n\t"
    "v_mul_f32_e32 v2, v2, v245\n\t"
    "v_mul_f32_e32 v3, v3, v245\n\t"
    "v_cvt_pk_bf16_f32 v4, v0, v1\n\t"
    "v_cvt_pk_bf16_f32 v5, v2, v3\n\t"
    "v_xor_b32_e32 v6, 96, v210\n\t"
    "v_add_u32_e32 v6, v6, v211\n\t"
    "ds_write_b64 v6, v[4:5] offset:8192\n\t"
    "v_accvgpr_read_b32 v0, a60\n\t"
    "v_accvgpr_read_b32 v1, a61\n\t"
    "v_accvgpr_read_b32 v2, a62\n\t"
    "v_accvgpr_read_b32 v3, a63\n\t"
    "v_mul_f32_e32 v0, v0, v245\n\t"
    "v_mul_f32_e32 v1, v1, v245\n\t"
    "v_mul_f32_e32 v2, v2, v245\n\t"
    "v_mul_f32_e32 v3, v3, v245\n\t"
    "v_cvt_pk_bf16_f32 v4, v0, v1\n\t"
    "v_cvt_pk_bf16_f32 v5, v2, v3\n\t"
    "v_xor_b32_e32 v6, 112, v210\n\t"
    "v_add_u32_e32 v6, v6, v211\n\t"
    "ds_write_b64 v6, v[4:5] offset:8192\n\t"
    "v_accvgpr_read_b32 v0, a80\n\t"
    "v_accvgpr_read_b32 v1, a81\n\t"
    "v_accvgpr_read_b32 v2, a82\n\t"
    "v_accvgpr_read_b32 v3, a83\n\t"
    "v_mul_f32_e32 v0, v0, v245\n\t"
    "v_mul_f32_e32 v1, v1, v245\n\t"
    "v_mul_f32_e32 v2, v2, v245\n\t"
    "v_mul_f32_e32 v3, v3, v245\n\t"
    "v_cvt_pk_bf16_f32 v4, v0, v1\n\t"
    "v_cvt_pk_bf16_f32 v5, v2, v3\n\t"
    "v_xor_b32_e32 v6, 128, v210\n\t"
    "v_add_u32_e32 v6, v6, v211\n\t"
    "ds_write_b64 v6, v[4:5] offset:8192\n\t"
    "v_accvgpr_read_b32 v0, a84\n\t"
    "v_accvgpr_read_b32 v1, a85\n\t"
    "v_accvgpr_read_b32 v2, a86\n\t"
    "v_accvgpr_read_b32 v3, a87\n\t"
    "v_mul_f32_e32 v0, v0, v245\n\t"
    "v_mul_f32_e32 v1, v1, v245\n\t"
    "v_mul_f32_e32 v2, v2, v245\n\t"
    "v_mul_f32_e32 v3, v3, v245\n\t"
    "v_cvt_pk_bf16_f32 v4, v0, v1\n\t"
    "v_cvt_pk_bf16_f32 v5, v2, v3\n\t"
    "v_xor_b32_e32 v6, 144, v210\n\t"
    "v_add_u32_e32 v6, v6, v211\n\t"
    "ds_write_b64 v6, v[4:5] offset:8192\n\t"
    "v_accvgpr_read_b32 v0, a88\n\t"
    "v_accvgpr_read_b32 v1, a89\n\t"
    "v_accvgpr_read_b32 v2, a90\n\t"
    "v_accvgpr_read_b32 v3, a91\n\t"
    "v_mul_f32_e32 v0, v0, v245\n\t"
    "v_mul_f32_e32 v1, v1, v245\n\t"
    "v_mul_f32_e32 v2, v2, v245\n\t"
    "v_mul_f32_e32 v3, v3, v245\n\t"
    "v_cvt_pk_bf16_f32 v4, v0, v1\n\t"
    "v_cvt_pk_bf16_f32 v5, v2, v3\n\t"
    "v_xor_b32_e32 v6, 160, v210\n\t"
    "v_add_u32_e32 v6, v6, v211\n\t"
    "ds_write_b64 v6, v[4:5] offset:8192\n\t"
    "v_accvgpr_read_b32 v0, a92\n\t"
    "v_accvgpr_read_b32 v1, a93\n\t"
    "v_accvgpr_read_b32 v2, a94\n\t"
    "v_accvgpr_read_b32 v3, a95\n\t"
    "v_mul_f32_e32 v0, v0, v245\n\t"
    "v_mul_f32_e32 v1, v1, v245\n\t"
    "v_mul_f32_e32 v2, v2, v245\n\t"
    "v_mul_f32_e32 v3, v3, v245\n\t"
    "v_cvt_pk_bf16_f32 v4, v0, v1\n\t"
    "v_cvt_pk_bf16_f32 v5, v2, v3\n\t"
    "v_xor_b32_e32 v6, 176, v210\n\t"
    "v_add_u32_e32 v6, v6, v211\n\t"
    "ds_write_b64 v6, v[4:5] offset:8192\n\t"
    "v_accvgpr_read_b32 v0, a112\n\t"
    "v_accvgpr_read_b32 v1, a113\n\t"
    "v_accvgpr_read_b32 v2, a114\n\t"
    "v_accvgpr_read_b32 v3, a115\n\t"
    "v_mul_f32_e32 v0, v0, v245\n\t"
    "v_mul_f32_e32 v1, v1, v245\n\t"
    "v_mul_f32_e32 v2, v2, v245\n\t"
    "v_mul_f32_e32 v3, v3, v245\n\t"
    "v_cvt_pk_bf16_f32 v4, v0, v1\n\t"
    "v_cvt_pk_bf16_f32 v5, v2, v3\n\t"
    "v_xor_b32_e32 v6, 192, v210\n\t"
    "v_add_u32_e32 v6, v6, v211\n\t"
    "ds_write_b64 v6, v[4:5] offset:8192\n\t"
    "v_accvgpr_read_b32 v0, a116\n\t"
    "v_accvgpr_read_b32 v1, a117\n\t"
    "v_accvgpr_read_b32 v2, a118\n\t"
    "v_accvgpr_read_b32 v3, a119\n\t"
    "v_mul_f32_e32 v0, v0, v245\n\t"
    "v_mul_f32_e32 v1, v1, v245\n\t"
    "v_mul_f32_e32 v2, v2, v245\n\t"
    "v_mul_f32_e32 v3, v3, v245\n\t"
    "v_cvt_pk_bf16_f32 v4, v0, v1\n\t"
    "v_cvt_pk_bf16_f32 v5, v2, v3\n\t"
    "v_xor_b32_e32 v6, 208, v210\n\t"
    "v_add_u32_e32 v6, v6, v211\n\t"
    "ds_write_b64 v6, v[4:5] offset:8192\n\t"
    "v_accvgpr_read_b32 v0, a120\n\t"
    "v_accvgpr_read_b32 v1, a121\n\t"
    "v_accvgpr_read_b32 v2, a122\n\t"
    "v_accvgpr_read_b32 v3, a123\n\t"
    "v_mul_f32_e32 v0, v0, v245\n\t"
    "v_mul_f32_e32 v1, v1, v245\n\t"
    "v_mul_f32_e32 v2, v2, v245\n\t"
    "v_mul_f32_e32 v3, v3, v245\n\t"
    "v_cvt_pk_bf16_f32 v4, v0, v1\n\t"
    "v_cvt_pk_bf16_f32 v5, v2, v3\n\t"
    "v_xor_b32_e32 v6, 224, v210\n\t"
    "v_add_u32_e32 v6, v6, v211\n\t"
    "ds_write_b64 v6, v[4:5] offset:8192\n\t"
    "v_accvgpr_read_b32 v0, a124\n\t"
    "v_accvgpr_read_b32 v1, a125\n\t"
    "v_accvgpr_read_b32 v2, a126\n\t"
    "v_accvgpr_read_b32 v3, a127\n\t"
    "v_mul_f32_e32 v0, v0, v245\n\t"
    "v_mul_f32_e32 v1, v1, v245\n\t"
    "v_mul_f32_e32 v2, v2, v245\n\t"
    "v_mul_f32_e32 v3, v3, v245\n\t"
    "v_cvt_pk_bf16_f32 v4, v0, v1\n\t"
    "v_cvt_pk_bf16_f32 v5, v2, v3\n\t"
    "v_xor_b32_e32 v6, 240, v210\n\t"
    "v_add_u32_e32 v6, v6, v211\n\t"
    "ds_write_b64 v6, v[4:5] offset:8192\n\t"
    "s_waitcnt lgkmcnt(0)\n\t"
    "ds_read_b128 v[8:11], v212 offset:0\n\t"
    "ds_read_b128 v[12:15], v213 offset:1024\n\t"
    "ds_read_b128 v[16:19], v214 offset:2048\n\t"
    "ds_read_b128 v[20:23], v215 offset:3072\n\t"
    "ds_read_b128 v[24:27], v212 offset:4096\n\t"
    "ds_read_b128 v[28:31], v213 offset:5120\n\t"
    "ds_read_b128 v[32:35], v214 offset:6144\n\t"
    "ds_read_b128 v[36:39], v215 offset:7168\n\t"
    "ds_read_b128 v[40:43], v212 offset:8192\n\t"
    "ds_read_b128 v[44:47], v213 offset:9216\n\t"
    "ds_read_b128 v[48:51], v214 offset:10240\n\t"
    "ds_read_b128 v[52:55], v215 offset:11264\n\t"
    "ds_read_b128 v[56:59], v212 offset:12288\n\t"
    "ds_read_b128 v[60:63], v213 offset:13312\n\t"
    "ds_read_b128 v[64:67], v214 offset:14336\n\t"
    "ds_read_b128 v[68:71], v215 offset:15360\n\t"
    "s_waitcnt lgkmcnt(0)\n\t"
    "buffer_store_dwordx4 v[8:11], v216, %[srdO], %[soo] offen offset:0\n\t"
    "buffer_store_dwordx4 v[12:15], v216, %[srdO], %[soo] offen offset:1024\n\t"
    "buffer_store_dwordx4 v[16:19], v216, %[srdO], %[soo] offen offset:2048\n\t"
    "buffer_store_dwordx4 v[20:23], v216, %[srdO], %[soo] offen offset:3072\n\t"
    "buffer_store_dwordx4 v[24:27], v216, %[srdO], %[soo1] offen offset:0\n\t"
    "buffer_store_dwordx4 v[28:31], v216, %[srdO], %[soo1] offen offset:1024\n\t"
    "buffer_store_dwordx4 v[32:35], v216, %[srdO], %[soo1] offen offset:2048\n\t"
    "buffer_store_dwordx4 v[36:39], v216, %[srdO], %[soo1] offen offset:3072\n\t"
    "buffer_store_dwordx4 v[40:43], v216, %[srdO], %[soo2] offen offset:0\n\t"
    "buffer_store_dwordx4 v[44:47], v216, %[srdO], %[soo2] offen offset:1024\n\t"
    "buffer_store_dwordx4 v[48:51], v216, %[srdO], %[soo2] offen offset:2048\n\t"
    "buffer_store_dwordx4 v[52:55], v216, %[srdO], %[soo2] offen offset:3072\n\t"
    "buffer_store_dwordx4 v[56:59], v216, %[srdO], %[soo3] offen offset:0\n\t"
    "buffer_store_dwordx4 v[60:63], v216, %[srdO], %[soo3] offen offset:1024\n\t"
    "buffer_store_dwordx4 v[64:67], v216, %[srdO], %[soo3] offen offset:2048\n\t"
    "buffer_store_dwordx4 v[68:71], v216, %[srdO], %[soo3] offen offset:3072\n\t"
    "s_branch END_%=\n\t"
    "RARE_0_%=:\n\t"
    "s_sub_i32 s58, s50, %[skbias]\n\t"
    "s_add_i32 s59, s58, 8192\n\t"
    "buffer_load_dwordx4 v[160:163], v248, %[srdK], s58 offen offset:0\n\t"
    "buffer_load_dwordx4 v[164:167], v248, %[srdK], s58 offen offset:32\n\t"
    "buffer_load_dwordx4 v[168:171], v248, %[srdK], s58 offen offset:64\n\t"
    "buffer_load_dwordx4 v[172:175], v248, %[srdK], s58 offen offset:96\n\t"
    "buffer_load_dwordx4 v[176:179], v248, %[srdK], s58 offen offset:128\n\t"
    "buffer_load_dwordx4 v[180:183], v248, %[srdK], s58 offen offset:160\n\t"
    "buffer_load_dwordx4 v[184:187], v248, %[srdK], s58 offen offset:192\n\t"
    "buffer_load_dwordx4 v[188:191], v248, %[srdK], s58 offen offset:224\n\t"
    "buffer_load_dwordx4 v[192:195], v248, %[srdK], s59 offen offset:0\n\t"
    "buffer_load_dwordx4 v[196:199], v248, %[srdK], s59 offen offset:32\n\t"
    "buffer_load_dwordx4 v[200:203], v248, %[srdK], s59 offen offset:64\n\t"
    "buffer_load_dwordx4 v[204:207], v248, %[srdK], s59 offen offset:96\n\t"
    "buffer_load_dwordx4 v[208:211], v248, %[srdK], s59 offen offset:128\n\t"
    "buffer_load_dwordx4 v[212:215], v248, %[srdK], s59 offen offset:160\n\t"
    "buffer_load_dwordx4 v[216:219], v248, %[srdK], s59 offen offset:192\n\t"
    "buffer_load_dwordx4 v[220:223], v248, %[srdK], s59 offen offset:224\n\t"
    "s_waitcnt vmcnt(0)\n\t"
    "v_mfma_f32_32x32x16_bf16 v[0:15], v[160:163], a[128:131], v[128:143]\n\t"
    "v_mfma_f32_32x32x16_bf16 v[16:31], v[160:163], a[160:163], v[144:159]\n\t"
    "v_mfma_f32_32x32x16_bf16 v[0:15], v[164:167], a[132:135], v[0:15]\n\t"
    "v_mfma_f32_32x32x16_bf16 v[16:31], v[164:167], a[164:167], v[16:31]\n\t"
    "v_mfma_f32_32x32x16_bf16 v[0:15], v[168:171], a[136:139], v[0:15]\n\t"
    "v_mfma_f32_32x32x16_bf16 v[16:31], v[168:171], a[168:171], v[16:31]\n\t"
    "v_mfma_f32_32x32x16_bf16 v[0:15], v[172:175], a[140:143], v[0:15]\n\t"
    "v_mfma_f32_32x32x16_bf16 v[16:31], v[172:175], a[172:175], v[16:31]\n\t"
    "v_mfma_f32_32x32x16_bf16 v[0:15], v[176:179], a[144:147], v[0:15]\n\t"
    "v_mfma_f32_32x32x16_bf16 v[16:31], v[176:179], a[176:179], v[16:31]\n\t"
    "v_mfma_f32_32x32x16_bf16 v[0:15], v[180:183], a[148:151], v[0:15]\n\t"
    "v_mfma_f32_32x32x16_bf16 v[16:31], v[180:183], a[180:183], v[16:31]\n\t"
    "v_mfma_f32_32x32x16_bf16 v[0:15], v[184:187], a[152:155], v[0:15]\n\t"
    "v_mfma_f32_32x32x16_bf16 v[16:31], v[184:187], a[184:187], v[16:31]\n\t"
    "v_mfma_f32_32x32x16_bf16 v[0:15], v[188:191], a[156:159], v[0:15]\n\t"
    "v_mfma_f32_32x32x16_bf16 v[16:31], v[188:191], a[188:191], v[16:31]\n\t"
    "v_mfma_f32_32x32x16_bf16 v[32:47], v[192:195], a[128:131], v[128:143]\n\t"
    "v_mfma_f32_32x32x16_bf16 v[48:63], v[192:195], a[160:163], v[144:159]\n\t"
    "v_mfma_f32_32x32x16_bf16 v[32:47], v[196:199], a[132:135], v[32:47]\n\t"
    "v_mfma_f32_32x32x16_bf16 v[48:63], v[196:199], a[164:167], v[48:63]\n\t"
    "v_mfma_f32_32x32x16_bf16 v[32:47], v[200:203], a[136:139], v[32:47]\n\t"
    "v_mfma_f32_32x32x16_bf16 v[48:63], v[200:203], a[168:171], v[48:63]\n\t"
    "v_mfma_f32_32x32x16_bf16 v[32:47], v[204:207], a[140:143], v[32:47]\n\t"
    "v_mfma_f32_32x32x16_bf16 v[48:63], v[204:207], a[172:175], v[48:63]\n\t"
    "v_mfma_f32_32x32x16_bf16 v[32:47], v[208:211], a[144:147], v[32:47]\n\t"
    "v_mfma_f32_32x32x16_bf16 v[48:63], v[208:211], a[176:179], v[48:63]\n\t"
    "v_mfma_f32_32x32x16_bf16 v[32:47], v[212:215], a[148:151], v[32:47]\n\t"
    "v_mfma_f32_32x32x16_bf16 v[48:63], v[212:215], a[180:183], v[48:63]\n\t"
    "v_mfma_f32_32x32x16_bf16 v[32:47], v[216:219], a[152:155], v[32:47]\n\t"
    "v_mfma_f32_32x32x16_bf16 v[48:63], v[216:219], a[184:187], v[48:63]\n\t"
    "v_mfma_f32_32x32x16_bf16 v[32:47], v[220:223], a[156:159], v[32:47]\n\t"
    "v_mfma_f32_32x32x16_bf16 v[48:63], v[220:223], a[188:191], v[48:63]\n\t"
    "s_nop 7\n\t"
    "s_nop 7\n\t"
    "v_max3_f32 v242, v0, v1, v2\n\t"
    "v_max3_f32 v242, v242, v3, v4\n\t"
    "v_max3_f32 v242, v242, v5, v6\n\t"
    "v_max3_f32 v242, v242, v7, v8\n\t"
    "v_max3_f32 v242, v242, v9, v10\n\t"
    "v_max3_f32 v242, v242, v11, v12\n\t"
    "v_max3_f32 v242, v242, v13, v14\n\t"
    "v_max3_f32 v242, v242, v15, v32\n\t"
    "v_max3_f32 v242, v242, v33, v34\n\t"
    "v_max3_f32 v242, v242, v35, v36\n\t"
    "v_max3_f32 v242, v242, v37, v38\n\t"
    "v_max3_f32 v242, v242, v39, v40\n\t"
    "v_max3_f32 v242, v242, v41, v42\n\t"
    "v_max3_f32 v242, v242, v43, v44\n\t"
    "v_max3_f32 v242, v242, v45, v46\n\t"
    "v_max_f32_e32 v242, v242, v47\n\t"
    "v_mov_b32_e32 v243, v242\n\t"
    "s_nop 1\n\t"
    "v_permlane32_swap_b32_e32 v242, v243\n\t"
    "v_max_f32_e32 v242, v242, v243\n\t"
    "v_max_f32_e32 v242, 0, v242\n\t"
    "v_add_f32_e32 v240, v240, v242\n\t"
    "v_sub_f32_e32 v244, 0, v242\n\t"
    "v_exp_f32_e32 v244, v244\n\t"
    "v_sub_f32_e32 v0, v0, v242\n\t"
    "v_sub_f32_e32 v1, v1, v242\n\t"
    "v_sub_f32_e32 v2, v2, v242\n\t"
    "v_sub_f32_e32 v3, v3, v242\n\t"
    "v_sub_f32_e32 v4, v4, v242\n\t"
    "v_sub_f32_e32 v5, v5, v242\n\t"
    "v_sub_f32_e32 v6, v6, v242\n\t"
    "v_sub_f32_e32 v7, v7, v242\n\t"
    "v_sub_f32_e32 v8, v8, v242\n\t"
    "v_sub_f32_e32 v9, v9, v242\n\t"
    "v_sub_f32_e32 v10, v10, v242\n\t"
    "v_sub_f32_e32 v11, v11, v242\n\t"
    "v_sub_f32_e32 v12, v12, v242\n\t"
    "v_sub_f32_e32 v13, v13, v242\n\t"
    "v_sub_f32_e32 v14, v14, v242\n\t"
    "v_sub_f32_e32 v15, v15, v242\n\t"
    "v_sub_f32_e32 v32, v32, v242\n\t"
    "v_sub_f32_e32 v33, v33, v242\n\t"
    "v_sub_f32_e32 v34, v34, v242\n\t"
    "v_sub_f32_e32 v35, v35, v242\n\t"
    "v_sub_f32_e32 v36, v36, v242\n\t"
    "v_sub_f32_e32 v37, v37, v242\n\t"
    "v_sub_f32_e32 v38, v38, v242\n\t"
    "v_sub_f32_e32 v39, v39, v242\n\t"
    "v_sub_f32_e32 v40, v40, v242\n\t"
    "v_sub_f32_e32 v41, v41, v242\n\t"
    "v_sub_f32_e32 v42, v42, v242\n\t"
    "v_sub_f32_e32 v43, v43, v242\n\t"
    "v_sub_f32_e32 v44, v44, v242\n\t"
    "v_sub_f32_e32 v45, v45, v242\n\t"
    "v_sub_f32_e32 v46, v46, v242\n\t"
    "v_sub_f32_e32 v47, v47, v242\n\t"
    "v_sub_f32_e32 v64, v64, v242\n\t"
    "v_sub_f32_e32 v65, v65, v242\n\t"
    "v_sub_f32_e32 v66, v66, v242\n\t"
    "v_sub_f32_e32 v67, v67, v242\n\t"
    "v_sub_f32_e32 v68, v68, v242\n\t"
    "v_sub_f32_e32 v69, v69, v242\n\t"
    "v_sub_f32_e32 v70, v70, v242\n\t"
    "v_sub_f32_e32 v71, v71, v242\n\t"
    "v_sub_f32_e32 v72, v72, v242\n\t"
    "v_sub_f32_e32 v73, v73, v242\n\t"
    "v_sub_f32_e32 v74, v74, v242\n\t"
    "v_sub_f32_e32 v75, v75, v242\n\t"
    "v_sub_f32_e32 v76, v76, v242\n\t"
    "v_sub_f32_e32 v77, v77, v242\n\t"
    "v_sub_f32_e32 v78, v78, v242\n\t"
    "v_sub_f32_e32 v79, v79, v242\n\t"
    "v_sub_f32_e32 v96, v96, v242\n\t"
    "v_sub_f32_e32 v97, v97, v242\n\t"
    "v_sub_f32_e32 v98, v98, v242\n\t"
    "v_sub_f32_e32 v99, v99, v242\n\t"
    "v_sub_f32_e32 v100, v100, v242\n\t"
    "v_sub_f32_e32 v101, v101, v242\n\t"
    "v_sub_f32_e32 v102, v102, v242\n\t"
    "v_sub_f32_e32 v103, v103, v242\n\t"
    "v_sub_f32_e32 v104, v104, v242\n\t"
    "v_sub_f32_e32 v105, v105, v242\n\t"
    "v_sub_f32_e32 v106, v106, v242\n\t"
    "v_sub_f32_e32 v107, v107, v242\n\t"
    "v_sub_f32_e32 v108, v108, v242\n\t"
    "v_sub_f32_e32 v109, v109, v242\n\t"
    "v_sub_f32_e32 v110, v110, v242\n\t"
    "v_sub_f32_e32 v111, v111, v242\n\t"
    "v_sub_f32_e32 v243, 0, v240\n\t"
    "v_mov_b32_e32 v128, v243\n\t"
    "v_mov_b32_e32 v129, v243\n\t"
    "v_mov_b32_e32 v130, v243\n\t"
    "v_mov_b32_e32 v131, v243\n\t"
    "v_mov_b32_e32 v132, v243\n\t"
    "v_mov_b32_e32 v133, v243\n\t"
    "v_mov_b32_e32 v134, v243\n\t"
    "v_mov_b32_e32 v135, v243\n\t"
    "v_mov_b32_e32 v136, v243\n\t"
    "v_mov_b32_e32 v137, v243\n\t"
    "v_mov_b32_e32 v138, v243\n\t"
    "v_mov_b32_e32 v139, v243\n\t"
    "v_mov_b32_e32 v140, v243\n\t"
    "v_mov_b32_e32 v141, v243\n\t"
    "v_mov_b32_e32 v142, v243\n\t"
    "v_mov_b32_e32 v143, v243\n\t"
    "v_mul_f32_e32 v236, v236, v244\n\t"
    "v_mul_f32_e32 v237, v237, v244\n\t"
    "v_max3_f32 v242, v16, v17, v18\n\t"
    "v_max3_f32 v242, v242, v19, v20\n\t"
    "v_max3_f32 v242, v242, v21, v22\n\t"
    "v_max3_f32 v242, v242, v23, v24\n\t"
    "v_max3_f32 v242, v242, v25, v26\n\t"
    "v_max3_f32 v242, v242, v27, v28\n\t"
    "v_max3_f32 v242, v242, v29, v30\n\t"
    "v_max3_f32 v242, v242, v31, v48\n\t"
    "v_max3_f32 v242, v242, v49, v50\n\t"
    "v_max3_f32 v242, v242, v51, v52\n\t"
    "v_max3_f32 v242, v242, v53, v54\n\t"
    "v_max3_f32 v242, v242, v55, v56\n\t"
    "v_max3_f32 v242, v242, v57, v58\n\t"
    "v_max3_f32 v242, v242, v59, v60\n\t"
    "v_max3_f32 v242, v242, v61, v62\n\t"
    "v_max_f32_e32 v242, v242, v63\n\t"
    "v_mov_b32_e32 v243, v242\n\t"
    "s_nop 1\n\t"
    "v_permlane32_swap_b32_e32 v242, v243\n\t"
    "v_max_f32_e32 v242, v242, v243\n\t"
    "v_max_f32_e32 v242, 0, v242\n\t"
    "v_add_f32_e32 v241, v241, v242\n\t"
    "v_sub_f32_e32 v245, 0, v242\n\t"
    "v_exp_f32_e32 v245, v245\n\t"
    "v_sub_f32_e32 v16, v16, v242\n\t"
    "v_sub_f32_e32 v17, v17, v242\n\t"
    "v_sub_f32_e32 v18, v18, v242\n\t"
    "v_sub_f32_e32 v19, v19, v242\n\t"
    "v_sub_f32_e32 v20, v20, v242\n\t"
    "v_sub_f32_e32 v21, v21, v242\n\t"
    "v_sub_f32_e32 v22, v22, v242\n\t"
    "v_sub_f32_e32 v23, v23, v242\n\t"
    "v_sub_f32_e32 v24, v24, v242\n\t"
    "v_sub_f32_e32 v25, v25, v242\n\t"
    "v_sub_f32_e32 v26, v26, v242\n\t"
    "v_sub_f32_e32 v27, v27, v242\n\t"
    "v_sub_f32_e32 v28, v28, v242\n\t"
    "v_sub_f32_e32 v29, v29, v242\n\t"
    "v_sub_f32_e32 v30, v30, v242\n\t"
    "v_sub_f32_e32 v31, v31, v242\n\t"
    "v_sub_f32_e32 v48, v48, v242\n\t"
    "v_sub_f32_e32 v49, v49, v242\n\t"
    "v_sub_f32_e32 v50, v50, v242\n\t"
    "v_sub_f32_e32 v51, v51, v242\n\t"
    "v_sub_f32_e32 v52, v52, v242\n\t"
    "v_sub_f32_e32 v53, v53, v242\n\t"
    "v_sub_f32_e32 v54, v54, v242\n\t"
    "v_sub_f32_e32 v55, v55, v242\n\t"
    "v_sub_f32_e32 v56, v56, v242\n\t"
    "v_sub_f32_e32 v57, v57, v242\n\t"
    "v_sub_f32_e32 v58, v58, v242\n\t"
    "v_sub_f32_e32 v59, v59, v242\n\t"
    "v_sub_f32_e32 v60, v60, v242\n\t"
    "v_sub_f32_e32 v61, v61, v242\n\t"
    "v_sub_f32_e32 v62, v62, v242\n\t"
    "v_sub_f32_e32 v63, v63, v242\n\t"
    "v_sub_f32_e32 v80, v80, v242\n\t"
    "v_sub_f32_e32 v81, v81, v242\n\t"
    "v_sub_f32_e32 v82, v82, v242\n\t"
    "v_sub_f32_e32 v83, v83, v242\n\t"
    "v_sub_f32_e32 v84, v84, v242\n\t"
    "v_sub_f32_e32 v85, v85, v242\n\t"
    "v_sub_f32_e32 v86, v86, v242\n\t"
    "v_sub_f32_e32 v87, v87, v242\n\t"
    "v_sub_f32_e32 v88, v88, v242\n\t"
    "v_sub_f32_e32 v89, v89, v242\n\t"
    "v_sub_f32_e32 v90, v90, v242\n\t"
    "v_sub_f32_e32 v91, v91, v242\n\t"
    "v_sub_f32_e32 v92, v92, v242\n\t"
    "v_sub_f32_e32 v93, v93, v242\n\t"
    "v_sub_f32_e32 v94, v94, v242\n\t"
    "v_sub_f32_e32 v95, v95, v242\n\t"
    "v_sub_f32_e32 v112, v112, v242\n\t"
    "v_sub_f32_e32 v113, v113, v242\n\t"
    "v_sub_f32_e32 v114, v114, v242\n\t"
    "v_sub_f32_e32 v115, v115, v242\n\t"
    "v_sub_f32_e32 v116, v116, v242\n\t"
    "v_sub_f32_e32 v117, v117, v242\n\t"
    "v_sub_f32_e32 v118, v118, v242\n\t"
    "v_sub_f32_e32 v119, v119, v242\n\t"
    "v_sub_f32_e32 v120, v120, v242\n\t"
    "v_sub_f32_e32 v121, v121, v242\n\t"
    "v_sub_f32_e32 v122, v122, v242\n\t"
    "v_sub_f32_e32 v123, v123, v242\n\t"
    "v_sub_f32_e32 v124, v124, v242\n\t"
    "v_sub_f32_e32 v125, v125, v242\n\t"
    "v_sub_f32_e32 v126, v126, v242\n\t"
    "v_sub_f32_e32 v127, v127, v242\n\t"
    "v_sub_f32_e32 v243, 0, v241\n\t"
    "v_mov_b32_e32 v144, v243\n\t"
    "v_mov_b32_e32 v145, v243\n\t"
    "v_mov_b32_e32 v146, v243\n\t"
    "v_mov_b32_e32 v147, v243\n\t"
    "v_mov_b32_e32 v148, v243\n\t"
    "v_mov_b32_e32 v149, v243\n\t"
    "v_mov_b32_e32 v150, v243\n\t"
    "v_mov_b32_e32 v151, v243\n\t"
    "v_mov_b32_e32 v152, v243\n\t"
    "v_mov_b32_e32 v153, v243\n\t"
    "v_mov_b32_e32 v154, v243\n\t"
    "v_mov_b32_e32 v155, v243\n\t"
    "v_mov_b32_e32 v156, v243\n\t"
    "v_mov_b32_e32 v157, v243\n\t"
    "v_mov_b32_e32 v158, v243\n\t"
    "v_mov_b32_e32 v159, v243\n\t"
    "v_mul_f32_e32 v238, v238, v245\n\t"
    "v_mul_f32_e32 v239, v239, v245\n\t"
    "v_accvgpr_read_b32 v246, a0\n\t"
    "v_mul_f32_e32 v246, v246, v244\n\t"
    "v_accvgpr_write_b32 a0, v246\n\t"
    "v_accvgpr_read_b32 v246, a1\n\t"
    "v_mul_f32_e32 v246, v246, v244\n\t"
    "v_accvgpr_write_b32 a1, v246\n\t"
    "v_accvgpr_read_b32 v246, a2\n\t"
    "v_mul_f32_e32 v246, v246, v244\n\t"
    "v_accvgpr_write_b32 a2, v246\n\t"
    "v_accvgpr_read_b32 v246, a3\n\t"
    "v_mul_f32_e32 v246, v246, v244\n\t"
    "v_accvgpr_write_b32 a3, v246\n\t"
    "v_accvgpr_read_b32 v246, a4\n\t"
    "v_mul_f32_e32 v246, v246, v244\n\t"
    "v_accvgpr_write_b32 a4, v246\n\t"
    "v_accvgpr_read_b32 v246, a5\n\t"
    "v_mul_f32_e32 v246, v246, v244\n\t"
    "v_accvgpr_write_b32 a5, v246\n\t"
    "v_accvgpr_read_b32 v246, a6\n\t"
    "v_mul_f32_e32 v246, v246, v244\n\t"
    "v_accvgpr_write_b32 a6, v246\n\t"
    "v_accvgpr_read_b32 v246, a7\n\t"
    "v_mul_f32_e32 v246, v246, v244\n\t"
    "v_accvgpr_write_b32 a7, v246\n\t"
    "v_accvgpr_read_b32 v246, a8\n\t"
    "v_mul_f32_e32 v246, v246, v244\n\t"
    "v_accvgpr_write_b32 a8, v246\n\t"
    "v_accvgpr_read_b32 v246, a9\n\t"
    "v_mul_f32_e32 v246, v246, v244\n\t"
    "v_accvgpr_write_b32 a9, v246\n\t"
    "v_accvgpr_read_b32 v246, a10\n\t"
    "v_mul_f32_e32 v246, v246, v244\n\t"
    "v_accvgpr_write_b32 a10, v246\n\t"
    "v_accvgpr_read_b32 v246, a11\n\t"
    "v_mul_f32_e32 v246, v246, v244\n\t"
    "v_accvgpr_write_b32 a11, v246\n\t"
    "v_accvgpr_read_b32 v246, a12\n\t"
    "v_mul_f32_e32 v246, v246, v244\n\t"
    "v_accvgpr_write_b32 a12, v246\n\t"
    "v_accvgpr_read_b32 v246, a13\n\t"
    "v_mul_f32_e32 v246, v246, v244\n\t"
    "v_accvgpr_write_b32 a13, v246\n\t"
    "v_accvgpr_read_b32 v246, a14\n\t"
    "v_mul_f32_e32 v246, v246, v244\n\t"
    "v_accvgpr_write_b32 a14, v246\n\t"
    "v_accvgpr_read_b32 v246, a15\n\t"
    "v_mul_f32_e32 v246, v246, v244\n\t"
    "v_accvgpr_write_b32 a15, v246\n\t"
    "v_accvgpr_read_b32 v246, a16\n\t"
    "v_mul_f32_e32 v246, v246, v245\n\t"
    "v_accvgpr_write_b32 a16, v246\n\t"
    "v_accvgpr_read_b32 v246, a17\n\t"
    "v_mul_f32_e32 v246, v246, v245\n\t"
    "v_accvgpr_write_b32 a17, v246\n\t"
    "v_accvgpr_read_b32 v246, a18\n\t"
    "v_mul_f32_e32 v246, v246, v245\n\t"
    "v_accvgpr_write_b32 a18, v246\n\t"
    "v_accvgpr_read_b32 v246, a19\n\t"
    "v_mul_f32_e32 v246, v246, v245\n\t"
    "v_accvgpr_write_b32 a19, v246\n\t"
    "v_accvgpr_read_b32 v246, a20\n\t"
    "v_mul_f32_e32 v246, v246, v245\n\t"
    "v_accvgpr_write_b32 a20, v246\n\t"
    "v_accvgpr_read_b32 v246, a21\n\t"
    "v_mul_f32_e32 v246, v246, v245\n\t"
    "v_accvgpr_write_b32 a21, v246\n\t"
    "v_accvgpr_read_b32 v246, a22\n\t"
    "v_mul_f32_e32 v246, v246, v245\n\t"
    "v_accvgpr_write_b32 a22, v246\n\t"
    "v_accvgpr_read_b32 v246, a23\n\t"
    "v_mul_f32_e32 v246, v246, v245\n\t"
    "v_accvgpr_write_b32 a23, v246\n\t"
    "v_accvgpr_read_b32 v246, a24\n\t"
    "v_mul_f32_e32 v246, v246, v245\n\t"
    "v_accvgpr_write_b32 a24, v246\n\t"
    "v_accvgpr_read_b32 v246, a25\n\t"
    "v_mul_f32_e32 v246, v246, v245\n\t"
    "v_accvgpr_write_b32 a25, v246\n\t"
    "v_accvgpr_read_b32 v246, a26\n\t"
    "v_mul_f32_e32 v246, v246, v245\n\t"
    "v_accvgpr_write_b32 a26, v246\n\t"
    "v_accvgpr_read_b32 v246, a27\n\t"
    "v_mul_f32_e32 v246, v246, v245\n\t"
    "v_accvgpr_write_b32 a27, v246\n\t"
    "v_accvgpr_read_b32 v246, a28\n\t"
    "v_mul_f32_e32 v246, v246, v245\n\t"
    "v_accvgpr_write_b32 a28, v246\n\t"
    "v_accvgpr_read_b32 v246, a29\n\t"
    "v_mul_f32_e32 v246, v246, v245\n\t"
    "v_accvgpr_write_b32 a29, v246\n\t"
    "v_accvgpr_read_b32 v246, a30\n\t"
    "v_mul_f32_e32 v246, v246, v245\n\t"
    "v_accvgpr_write_b32 a30, v246\n\t"
    "v_accvgpr_read_b32 v246, a31\n\t"
    "v_mul_f32_e32 v246, v246, v245\n\t"
    "v_accvgpr_write_b32 a31, v246\n\t"
    "v_accvgpr_read_b32 v246, a32\n\t"
    "v_mul_f32_e32 v246, v246, v244\n\t"
    "v_accvgpr_write_b32 a32, v246\n\t"
    "v_accvgpr_read_b32 v246, a33\n\t"
    "v_mul_f32_e32 v246, v246, v244\n\t"
    "v_accvgpr_write_b32 a33, v246\n\t"
    "v_accvgpr_read_b32 v246, a34\n\t"
    "v_mul_f32_e32 v246, v246, v244\n\t"
    "v_accvgpr_write_b32 a34, v246\n\t"
    "v_accvgpr_read_b32 v246, a35\n\t"
    "v_mul_f32_e32 v246, v246, v244\n\t"
    "v_accvgpr_write_b32 a35, v246\n\t"
    "v_accvgpr_read_b32 v246, a36\n\t"
    "v_mul_f32_e32 v246, v246, v244\n\t"
    "v_accvgpr_write_b32 a36, v246\n\t"
    "v_accvgpr_read_b32 v246, a37\n\t"
    "v_mul_f32_e32 v246, v246, v244\n\t"
    "v_accvgpr_write_b32 a37, v246\n\t"
    "v_accvgpr_read_b32 v246, a38\n\t"
    "v_mul_f32_e32 v246, v246, v244\n\t"
    "v_accvgpr_write_b32 a38, v246\n\t"
    "v_accvgpr_read_b32 v246, a39\n\t"
    "v_mul_f32_e32 v246, v246, v244\n\t"
    "v_accvgpr_write_b32 a39, v246\n\t"
    "v_accvgpr_read_b32 v246, a40\n\t"
    "v_mul_f32_e32 v246, v246, v244\n\t"
    "v_accvgpr_write_b32 a40, v246\n\t"
    "v_accvgpr_read_b32 v246, a41\n\t"
    "v_mul_f32_e32 v246, v246, v244\n\t"
    "v_accvgpr_write_b32 a41, v246\n\t"
    "v_accvgpr_read_b32 v246, a42\n\t"
    "v_mul_f32_e32 v246, v246, v244\n\t"
    "v_accvgpr_write_b32 a42, v246\n\t"
    "v_accvgpr_read_b32 v246, a43\n\t"
    "v_mul_f32_e32 v246, v246, v244\n\t"
    "v_accvgpr_write_b32 a43, v246\n\t"
    "v_accvgpr_read_b32 v246, a44\n\t"
    "v_mul_f32_e32 v246, v246, v244\n\t"
    "v_accvgpr_write_b32 a44, v246\n\t"
    "v_accvgpr_read_b32 v246, a45\n\t"
    "v_mul_f32_e32 v246, v246, v244\n\t"
    "v_accvgpr_write_b32 a45, v246\n\t"
    "v_accvgpr_read_b32 v246, a46\n\t"
    "v_mul_f32_e32 v246, v246, v244\n\t"
    "v_accvgpr_write_b32 a46, v246\n\t"
    "v_accvgpr_read_b32 v246, a47\n\t"
    "v_mul_f32_e32 v246, v246, v244\n\t"
    "v_accvgpr_write_b32 a47, v246\n\t"
    "v_accvgpr_read_b32 v246, a48\n\t"
    "v_mul_f32_e32 v246, v246, v245\n\t"
    "v_accvgpr_write_b32 a48, v246\n\t"
    "v_accvgpr_read_b32 v246, a49\n\t"
    "v_mul_f32_e32 v246, v246, v245\n\t"
    "v_accvgpr_write_b32 a49, v246\n\t"
    "v_accvgpr_read_b32 v246, a50\n\t"
    "v_mul_f32_e32 v246, v246, v245\n\t"
    "v_accvgpr_write_b32 a50, v246\n\t"
    "v_accvgpr_read_b32 v246, a51\n\t"
    "v_mul_f32_e32 v246, v246, v245\n\t"
    "v_accvgpr_write_b32 a51, v246\n\t"
    "v_accvgpr_read_b32 v246, a52\n\t"
    "v_mul_f32_e32 v246, v246, v245\n\t"
    "v_accvgpr_write_b32 a52, v246\n\t"
    "v_accvgpr_read_b32 v246, a53\n\t"
    "v_mul_f32_e32 v246, v246, v245\n\t"
    "v_accvgpr_write_b32 a53, v246\n\t"
    "v_accvgpr_read_b32 v246, a54\n\t"
    "v_mul_f32_e32 v246, v246, v245\n\t"
    "v_accvgpr_write_b32 a54, v246\n\t"
    "v_accvgpr_read_b32 v246, a55\n\t"
    "v_mul_f32_e32 v246, v246, v245\n\t"
    "v_accvgpr_write_b32 a55, v246\n\t"
    "v_accvgpr_read_b32 v246, a56\n\t"
    "v_mul_f32_e32 v246, v246, v245\n\t"
    "v_accvgpr_write_b32 a56, v246\n\t"
    "v_accvgpr_read_b32 v246, a57\n\t"
    "v_mul_f32_e32 v246, v246, v245\n\t"
    "v_accvgpr_write_b32 a57, v246\n\t"
    "v_accvgpr_read_b32 v246, a58\n\t"
    "v_mul_f32_e32 v246, v246, v245\n\t"
    "v_accvgpr_write_b32 a58, v246\n\t"
    "v_accvgpr_read_b32 v246, a59\n\t"
    "v_mul_f32_e32 v246, v246, v245\n\t"
    "v_accvgpr_write_b32 a59, v246\n\t"
    "v_accvgpr_read_b32 v246, a60\n\t"
    "v_mul_f32_e32 v246, v246, v245\n\t"
    "v_accvgpr_write_b32 a60, v246\n\t"
    "v_accvgpr_read_b32 v246, a61\n\t"
    "v_mul_f32_e32 v246, v246, v245\n\t"
    "v_accvgpr_write_b32 a61, v246\n\t"
    "v_accvgpr_read_b32 v246, a62\n\t"
    "v_mul_f32_e32 v246, v246, v245\n\t"
    "v_accvgpr_write_b32 a62, v246\n\t"
    "v_accvgpr_read_b32 v246, a63\n\t"
    "v_mul_f32_e32 v246, v246, v245\n\t"
    "v_accvgpr_write_b32 a63, v246\n\t"
    "v_accvgpr_read_b32 v246, a64\n\t"
    "v_mul_f32_e32 v246, v246, v244\n\t"
    "v_accvgpr_write_b32 a64, v246\n\t"
    "v_accvgpr_read_b32 v246, a65\n\t"
    "v_mul_f32_e32 v246, v246, v244\n\t"
    "v_accvgpr_write_b32 a65, v246\n\t"
    "v_accvgpr_read_b32 v246, a66\n\t"
    "v_mul_f32_e32 v246, v246, v244\n\t"
    "v_accvgpr_write_b32 a66, v246\n\t"
    "v_accvgpr_read_b32 v246, a67\n\t"
    "v_mul_f32_e32 v246, v246, v244\n\t"
    "v_accvgpr_write_b32 a67, v246\n\t"
    "v_accvgpr_read_b32 v246, a68\n\t"
    "v_mul_f32_e32 v246, v246, v244\n\t"
    "v_accvgpr_write_b32 a68, v246\n\t"
    "v_accvgpr_read_b32 v246, a69\n\t"
    "v_mul_f32_e32 v246, v246, v244\n\t"
    "v_accvgpr_write_b32 a69, v246\n\t"
    "v_accvgpr_read_b32 v246, a70\n\t"
    "v_mul_f32_e32 v246, v246, v244\n\t"
    "v_accvgpr_write_b32 a70, v246\n\t"
    "v_accvgpr_read_b32 v246, a71\n\t"
    "v_mul_f32_e32 v246, v246, v244\n\t"
    "v_accvgpr_write_b32 a71, v246\n\t"
    "v_accvgpr_read_b32 v246, a72\n\t"
    "v_mul_f32_e32 v246, v246, v244\n\t"
    "v_accvgpr_write_b32 a72, v246\n\t"
    "v_accvgpr_read_b32 v246, a73\n\t"
    "v_mul_f32_e32 v246, v246, v244\n\t"
    "v_accvgpr_write_b32 a73, v246\n\t"
    "v_accvgpr_read_b32 v246, a74\n\t"
    "v_mul_f32_e32 v246, v246, v244\n\t"
    "v_accvgpr_write_b32 a74, v246\n\t"
    "v_accvgpr_read_b32 v246, a75\n\t"
    "v_mul_f32_e32 v246, v246, v244\n\t"
    "v_accvgpr_write_b32 a75, v246\n\t"
    "v_accvgpr_read_b32 v246, a76\n\t"
    "v_mul_f32_e32 v246, v246, v244\n\t"
    "v_accvgpr_write_b32 a76, v246\n\t"
    "v_accvgpr_read_b32 v246, a77\n\t"
    "v_mul_f32_e32 v246, v246, v244\n\t"
    "v_accvgpr_write_b32 a77, v246\n\t"
    "v_accvgpr_read_b32 v246, a78\n\t"
    "v_mul_f32_e32 v246, v246, v244\n\t"
    "v_accvgpr_write_b32 a78, v246\n\t"
    "v_accvgpr_read_b32 v246, a79\n\t"
    "v_mul_f32_e32 v246, v246, v244\n\t"
    "v_accvgpr_write_b32 a79, v246\n\t"
    "v_accvgpr_read_b32 v246, a80\n\t"
    "v_mul_f32_e32 v246, v246, v245\n\t"
    "v_accvgpr_write_b32 a80, v246\n\t"
    "v_accvgpr_read_b32 v246, a81\n\t"
    "v_mul_f32_e32 v246, v246, v245\n\t"
    "v_accvgpr_write_b32 a81, v246\n\t"
    "v_accvgpr_read_b32 v246, a82\n\t"
    "v_mul_f32_e32 v246, v246, v245\n\t"
    "v_accvgpr_write_b32 a82, v246\n\t"
    "v_accvgpr_read_b32 v246, a83\n\t"
    "v_mul_f32_e32 v246, v246, v245\n\t"
    "v_accvgpr_write_b32 a83, v246\n\t"
    "v_accvgpr_read_b32 v246, a84\n\t"
    "v_mul_f32_e32 v246, v246, v245\n\t"
    "v_accvgpr_write_b32 a84, v246\n\t"
    "v_accvgpr_read_b32 v246, a85\n\t"
    "v_mul_f32_e32 v246, v246, v245\n\t"
    "v_accvgpr_write_b32 a85, v246\n\t"
    "v_accvgpr_read_b32 v246, a86\n\t"
    "v_mul_f32_e32 v246, v246, v245\n\t"
    "v_accvgpr_write_b32 a86, v246\n\t"
    "v_accvgpr_read_b32 v246, a87\n\t"
    "v_mul_f32_e32 v246, v246, v245\n\t"
    "v_accvgpr_write_b32 a87, v246\n\t"
    "v_accvgpr_read_b32 v246, a88\n\t"
    "v_mul_f32_e32 v246, v246, v245\n\t"
    "v_accvgpr_write_b32 a88, v246\n\t"
    "v_accvgpr_read_b32 v246, a89\n\t"
    "v_mul_f32_e32 v246, v246, v245\n\t"
    "v_accvgpr_write_b32 a89, v246\n\t"
    "v_accvgpr_read_b32 v246, a90\n\t"
    "v_mul_f32_e32 v246, v246, v245\n\t"
    "v_accvgpr_write_b32 a90, v246\n\t"
    "v_accvgpr_read_b32 v246, a91\n\t"
    "v_mul_f32_e32 v246, v246, v245\n\t"
    "v_accvgpr_write_b32 a91, v246\n\t"
    "v_accvgpr_read_b32 v246, a92\n\t"
    "v_mul_f32_e32 v246, v246, v245\n\t"
    "v_accvgpr_write_b32 a92, v246\n\t"
    "v_accvgpr_read_b32 v246, a93\n\t"
    "v_mul_f32_e32 v246, v246, v245\n\t"
    "v_accvgpr_write_b32 a93, v246\n\t"
    "v_accvgpr_read_b32 v246, a94\n\t"
    "v_mul_f32_e32 v246, v246, v245\n\t"
    "v_accvgpr_write_b32 a94, v246\n\t"
    "v_accvgpr_read_b32 v246, a95\n\t"
    "v_mul_f32_e32 v246, v246, v245\n\t"
    "v_accvgpr_write_b32 a95, v246\n\t"
    "v_accvgpr_read_b32 v246, a96\n\t"
    "v_mul_f32_e32 v246, v246, v244\n\t"
    "v_accvgpr_write_b32 a96, v246\n\t"
    "v_accvgpr_read_b32 v246, a97\n\t"
    "v_mul_f32_e32 v246, v246, v244\n\t"
    "v_accvgpr_write_b32 a97, v246\n\t"
    "v_accvgpr_read_b32 v246, a98\n\t"
    "v_mul_f32_e32 v246, v246, v244\n\t"
    "v_accvgpr_write_b32 a98, v246\n\t"
    "v_accvgpr_read_b32 v246, a99\n\t"
    "v_mul_f32_e32 v246, v246, v244\n\t"
    "v_accvgpr_write_b32 a99, v246\n\t"
    "v_accvgpr_read_b32 v246, a100\n\t"
    "v_mul_f32_e32 v246, v246, v244\n\t"
    "v_accvgpr_write_b32 a100, v246\n\t"
    "v_accvgpr_read_b32 v246, a101\n\t"
    "v_mul_f32_e32 v246, v246, v244\n\t"
    "v_accvgpr_write_b32 a101, v246\n\t"
    "v_accvgpr_read_b32 v246, a102\n\t"
    "v_mul_f32_e32 v246, v246, v244\n\t"
    "v_accvgpr_write_b32 a102, v246\n\t"
    "v_accvgpr_read_b32 v246, a103\n\t"
    "v_mul_f32_e32 v246, v246, v244\n\t"
    "v_accvgpr_write_b32 a103, v246\n\t"
    "v_accvgpr_read_b32 v246, a104\n\t"
    "v_mul_f32_e32 v246, v246, v244\n\t"
    "v_accvgpr_write_b32 a104, v246\n\t"
    "v_accvgpr_read_b32 v246, a105\n\t"
    "v_mul_f32_e32 v246, v246, v244\n\t"
    "v_accvgpr_write_b32 a105, v246\n\t"
    "v_accvgpr_read_b32 v246, a106\n\t"
    "v_mul_f32_e32 v246, v246, v244\n\t"
    "v_accvgpr_write_b32 a106, v246\n\t"
    "v_accvgpr_read_b32 v246, a107\n\t"
    "v_mul_f32_e32 v246, v246, v244\n\t"
    "v_accvgpr_write_b32 a107, v246\n\t"
    "v_accvgpr_read_b32 v246, a108\n\t"
    "v_mul_f32_e32 v246, v246, v244\n\t"
    "v_accvgpr_write_b32 a108, v246\n\t"
    "v_accvgpr_read_b32 v246, a109\n\t"
    "v_mul_f32_e32 v246, v246, v244\n\t"
    "v_accvgpr_write_b32 a109, v246\n\t"
    "v_accvgpr_read_b32 v246, a110\n\t"
    "v_mul_f32_e32 v246, v246, v244\n\t"
    "v_accvgpr_write_b32 a110, v246\n\t"
    "v_accvgpr_read_b32 v246, a111\n\t"
    "v_mul_f32_e32 v246, v246, v244\n\t"
    "v_accvgpr_write_b32 a111, v246\n\t"
    "v_accvgpr_read_b32 v246, a112\n\t"
    "v_mul_f32_e32 v246, v246, v245\n\t"
    "v_accvgpr_write_b32 a112, v246\n\t"
    "v_accvgpr_read_b32 v246, a113\n\t"
    "v_mul_f32_e32 v246, v246, v245\n\t"
    "v_accvgpr_write_b32 a113, v246\n\t"
    "v_accvgpr_read_b32 v246, a114\n\t"
    "v_mul_f32_e32 v246, v246, v245\n\t"
    "v_accvgpr_write_b32 a114, v246\n\t"
    "v_accvgpr_read_b32 v246, a115\n\t"
    "v_mul_f32_e32 v246, v246, v245\n\t"
    "v_accvgpr_write_b32 a115, v246\n\t"
    "v_accvgpr_read_b32 v246, a116\n\t"
    "v_mul_f32_e32 v246, v246, v245\n\t"
    "v_accvgpr_write_b32 a116, v246\n\t"
    "v_accvgpr_read_b32 v246, a117\n\t"
    "v_mul_f32_e32 v246, v246, v245\n\t"
    "v_accvgpr_write_b32 a117, v246\n\t"
    "v_accvgpr_read_b32 v246, a118\n\t"
    "v_mul_f32_e32 v246, v246, v245\n\t"
    "v_accvgpr_write_b32 a118, v246\n\t"
    "v_accvgpr_read_b32 v246, a119\n\t"
    "v_mul_f32_e32 v246, v246, v245\n\t"
    "v_accvgpr_write_b32 a119, v246\n\t"
    "v_accvgpr_read_b32 v246, a120\n\t"
    "v_mul_f32_e32 v246, v246, v245\n\t"
    "v_accvgpr_write_b32 a120, v246\n\t"
    "v_accvgpr_read_b32 v246, a121\n\t"
    "v_mul_f32_e32 v246, v246, v245\n\t"
    "v_accvgpr_write_b32 a121, v246\n\t"
    "v_accvgpr_read_b32 v246, a122\n\t"
    "v_mul_f32_e32 v246, v246, v245\n\t"
    "v_accvgpr_write_b32 a122, v246\n\t"
    "v_accvgpr_read_b32 v246, a123\n\t"
    "v_mul_f32_e32 v246, v246, v245\n\t"
    "v_accvgpr_write_b32 a123, v246\n\t"
    "v_accvgpr_read_b32 v246, a124\n\t"
    "v_mul_f32_e32 v246, v246, v245\n\t"
    "v_accvgpr_write_b32 a124, v246\n\t"
    "v_accvgpr_read_b32 v246, a125\n\t"
    "v_mul_f32_e32 v246, v246, v245\n\t"
    "v_accvgpr_write_b32 a125, v246\n\t"
    "v_accvgpr_read_b32 v246, a126\n\t"
    "v_mul_f32_e32 v246, v246, v245\n\t"
    "v_accvgpr_write_b32 a126, v246\n\t"
    "v_accvgpr_read_b32 v246, a127\n\t"
    "v_mul_f32_e32 v246, v246, v245\n\t"
    "v_accvgpr_write_b32 a127, v246\n\t"
    "v_exp_f32_e32 v0, v0\n\t"
    "s_nop 0\n\t"
    "v_exp_f32_e32 v1, v1\n\t"
    "s_nop 0\n\t"
    "v_exp_f32_e32 v2, v2\n\t"
    "s_nop 0\n\t"
    "v_add_f32_e32 v232, v0, v2\n\t"
    "v_exp_f32_e32 v3, v3\n\t"
    "s_nop 0\n\t"
    "v_add_f32_e32 v233, v1, v3\n\t"
    "v_exp_f32_e32 v4, v4\n\t"
    "s_nop 0\n\t"
    "v_add_f32_e32 v232, v232, v4\n\t"
    "v_exp_f32_e32 v5, v5\n\t"
    "s_nop 0\n\t"
    "v_add_f32_e32 v233, v233, v5\n\t"
    "v_exp_f32_e32 v6, v6\n\t"
    "s_nop 0\n\t"
    "v_add_f32_e32 v232, v232, v6\n\t"
    "v_exp_f32_e32 v7, v7\n\t"
    "s_nop 0\n\t"
    "v_add_f32_e32 v233, v233, v7\n\t"
    "v_exp_f32_e32 v16, v16\n\t"
    "s_nop 0\n\t"
    "v_exp_f32_e32 v17, v17\n\t"
    "s_nop 0\n\t"
    "v_exp_f32_e32 v18, v18\n\t"
    "s_nop 0\n\t"
    "v_add_f32_e32 v234, v16, v18\n\t"
    "v_exp_f32_e32 v19, v19\n\t"
    "s_nop 0\n\t"
    "v_add_f32_e32 v235, v17, v19\n\t"
    "v_exp_f32_e32 v20, v20\n\t"
    "s_nop 0\n\t"
    "v_add_f32_e32 v234, v234, v20\n\t"
    "v_exp_f32_e32 v21, v21\n\t"
    "s_nop 0\n\t"
    "v_add_f32_e32 v235, v235, v21\n\t"
    "v_exp_f32_e32 v22, v22\n\t"
    "s_nop 0\n\t"
    "v_add_f32_e32 v234, v234, v22\n\t"
    "v_exp_f32_e32 v23, v23\n\t"
    "s_nop 0\n\t"
    "v_add_f32_e32 v235, v235, v23\n\t"
    "v_exp_f32_e32 v8, v8\n\t"
    "s_nop 0\n\t"
    "v_add_f32_e32 v232, v232, v8\n\t"
    "v_exp_f32_e32 v9, v9\n\t"
    "s_nop 0\n\t"
    "v_add_f32_e32 v233, v233, v9\n\t"
    "v_exp_f32_e32 v10, v10\n\t"
    "s_nop 0\n\t"
    "v_add_f32_e32 v232, v232, v10\n\t"
    "v_exp_f32_e32 v11, v11\n\t"
    "s_nop 0\n\t"
    "v_add_f32_e32 v233, v233, v11\n\t"
    "v_exp_f32_e32 v12, v12\n\t"
    "s_nop 0\n\t"
    "v_add_f32_e32 v232, v232, v12\n\t"
    "v_exp_f32_e32 v13, v13\n\t"
    "s_nop 0\n\t"
    "v_add_f32_e32 v233, v233, v13\n\t"
    "v_exp_f32_e32 v14, v14\n\t"
    "s_nop 0\n\t"
    "v_add_f32_e32 v232, v232, v14\n\t"
    "v_exp_f32_e32 v15, v15\n\t"
    "s_nop 0\n\t"
    "v_add_f32_e32 v233, v233, v15\n\t"
    "v_exp_f32_e32 v24, v24\n\t"
    "s_nop 0\n\t"
    "v_add_f32_e32 v234, v234, v24\n\t"
    "v_exp_f32_e32 v25, v25\n\t"
    "s_nop 0\n\t"
    "v_add_f32_e32 v235, v235, v25\n\t"
    "v_exp_f32_e32 v26, v26\n\t"
    "s_nop 0\n\t"
    "v_add_f32_e32 v234, v234, v26\n\t"
    "v_exp_f32_e32 v27, v27\n\t"
    "s_nop 0\n\t"
    "v_add_f32_e32 v235, v235, v27\n\t"
    "v_exp_f32_e32 v28, v28\n\t"
    "s_nop 0\n\t"
    "v_add_f32_e32 v234, v234, v28\n\t"
    "v_exp_f32_e32 v29, v29\n\t"
    "s_nop 0\n\t"
    "v_add_f32_e32 v235, v235, v29\n\t"
    "v_exp_f32_e32 v30, v30\n\t"
    "s_nop 0\n\t"
    "v_add_f32_e32 v234, v234, v30\n\t"
    "v_exp_f32_e32 v31, v31\n\t"
    "s_nop 0\n\t"
    "v_add_f32_e32 v235, v235, v31\n\t"
    "v_exp_f32_e32 v32, v32\n\t"
    "s_nop 0\n\t"
    "v_add_f32_e32 v232, v232, v32\n\t"
    "v_exp_f32_e32 v33, v33\n\t"
    "s_nop 0\n\t"
    "v_add_f32_e32 v233, v233, v33\n\t"
    "v_exp_f32_e32 v34, v34\n\t"
    "s_nop 0\n\t"
    "v_add_f32_e32 v232, v232, v34\n\t"
    "v_exp_f32_e32 v35, v35\n\t"
    "s_nop 0\n\t"
    "v_add_f32_e32 v233, v233, v35\n\t"
    "v_exp_f32_e32 v36, v36\n\t"
    "s_nop 0\n\t"
    "v_add_f32_e32 v232, v232, v36\n\t"
    "v_exp_f32_e32 v37, v37\n\t"
    "s_nop 0\n\t"
    "v_add_f32_e32 v233, v233, v37\n\t"
    "v_exp_f32_e32 v38, v38\n\t"
    "s_nop 0\n\t"
    "v_add_f32_e32 v232, v232, v38\n\t"
    "v_exp_f32_e32 v39, v39\n\t"
    "s_nop 0\n\t"
    "v_add_f32_e32 v233, v233, v39\n\t"
    "v_exp_f32_e32 v48, v48\n\t"
    "s_nop 0\n\t"
    "v_add_f32_e32 v234, v234, v48\n\t"
    "v_exp_f32_e32 v49, v49\n\t"
    "s_nop 0\n\t"
    "v_add_f32_e32 v235, v235, v49\n\t"
    "v_exp_f32_e32 v50, v50\n\t"
    "s_nop 0\n\t"
    "v_add_f32_e32 v234, v234, v50\n\t"
    "v_exp_f32_e32 v51, v51\n\t"
    "s_nop 0\n\t"
    "v_add_f32_e32 v235, v235, v51\n\t"
    "v_exp_f32_e32 v52, v52\n\t"
    "s_nop 0\n\t"
    "v_add_f32_e32 v234, v234, v52\n\t"
    "v_exp_f32_e32 v53, v53\n\t"
    "s_nop 0\n\t"
    "v_add_f32_e32 v235, v235, v53\n\t"
    "v_exp_f32_e32 v54, v54\n\t"
    "s_nop 0\n\t"
    "v_add_f32_e32 v234, v234, v54\n\t"
    "v_exp_f32_e32 v55, v55\n\t"
    "s_nop 0\n\t"
    "v_add_f32_e32 v235, v235, v55\n\t"
    "v_exp_f32_e32 v40, v40\n\t"
    "s_nop 0\n\t"
    "v_add_f32_e32 v232, v232, v40\n\t"
    "v_exp_f32_e32 v41, v41\n\t"
    "s_nop 0\n\t"
    "v_add_f32_e32 v233, v233, v41\n\t"
    "v_exp_f32_e32 v42, v42\n\t"
    "s_nop 0\n\t"
    "v_add_f32_e32 v232, v232, v42\n\t"
    "v_exp_f32_e32 v43, v43\n\t"
    "s_nop 0\n\t"
    "v_add_f32_e32 v233, v233, v43\n\t"
    "v_exp_f32_e32 v44, v44\n\t"
    "s_nop 0\n\t"
    "v_add_f32_e32 v232, v232, v44\n\t"
    "v_exp_f32_e32 v45, v45\n\t"
    "s_nop 0\n\t"
    "v_add_f32_e32 v233, v233, v45\n\t"
    "v_exp_f32_e32 v46, v46\n\t"
    "s_nop 0\n\t"
    "v_add_f32_e32 v232, v232, v46\n\t"
    "v_exp_f32_e32 v47, v47\n\t"
    "s_nop 0\n\t"
    "v_add_f32_e32 v233, v233, v47\n\t"
    "v_exp_f32_e32 v56, v56\n\t"
    "s_nop 0\n\t"
    "v_add_f32_e32 v234, v234, v56\n\t"
    "v_exp_f32_e32 v57, v57\n\t"
    "s_nop 0\n\t"
    "v_add_f32_e32 v235, v235, v57\n\t"
    "v_exp_f32_e32 v58, v58\n\t"
    "s_nop 0\n\t"
    "v_add_f32_e32 v234, v234, v58\n\t"
    "v_exp_f32_e32 v59, v59\n\t"
    "s_nop 0\n\t"
    "v_add_f32_e32 v235, v235, v59\n\t"
    "v_exp_f32_e32 v60, v60\n\t"
    "s_nop 0\n\t"
    "v_add_f32_e32 v234, v234, v60\n\t"
    "v_exp_f32_e32 v61, v61\n\t"
    "s_nop 0\n\t"
    "v_add_f32_e32 v235, v235, v61\n\t"
    "v_exp_f32_e32 v62, v62\n\t"
    "s_nop 0\n\t"
    "v_add_f32_e32 v234, v234, v62\n\t"
    "v_exp_f32_e32 v63, v63\n\t"
    "s_nop 0\n\t"
    "v_add_f32_e32 v235, v235, v63\n\t"
    "v_cvt_pk_bf16_f32 v0, v0, v1\n\t"
    "v_cvt_pk_bf16_f32 v1, v2, v3\n\t"
    "v_cvt_pk_bf16_f32 v2, v4, v5\n\t"
    "v_cvt_pk_bf16_f32 v3, v6, v7\n\t"
    "v_cvt_pk_bf16_f32 v16, v16, v17\n\t"
    "v_cvt_pk_bf16_f32 v17, v18, v19\n\t"
    "v_cvt_pk_bf16_f32 v18, v20, v21\n\t"
    "v_cvt_pk_bf16_f32 v19, v22, v23\n\t"
    "v_cvt_pk_bf16_f32 v8, v8, v9\n\t"
    "v_cvt_pk_bf16_f32 v9, v10, v11\n\t"
    "v_cvt_pk_bf16_f32 v10, v12, v13\n\t"
    "v_cvt_pk_bf16_f32 v11, v14, v15\n\t"
    "v_cvt_pk_bf16_f32 v24, v24, v25\n\t"
    "v_cvt_pk_bf16_f32 v25, v26, v27\n\t"
    "v_cvt_pk_bf16_f32 v26, v28, v29\n\t"
    "v_cvt_pk_bf16_f32 v27, v30, v31\n\t"
    "v_cvt_pk_bf16_f32 v32, v32, v33\n\t"
    "v_cvt_pk_bf16_f32 v33, v34, v35\n\t"
    "v_cvt_pk_bf16_f32 v34, v36, v37\n\t"
    "v_cvt_pk_bf16_f32 v35, v38, v39\n\t"
    "v_cvt_pk_bf16_f32 v48, v48, v49\n\t"
    "v_cvt_pk_bf16_f32 v49, v50, v51\n\t"
    "v_cvt_pk_bf16_f32 v50, v52, v53\n\t"
    "v_cvt_pk_bf16_f32 v51, v54, v55\n\t"
    "v_cvt_pk_bf16_f32 v40, v40, v41\n\t"
    "v_cvt_pk_bf16_f32 v41, v42, v43\n\t"
    "v_cvt_pk_bf16_f32 v42, v44, v45\n\t"
    "v_cvt_pk_bf16_f32 v43, v46, v47\n\t"
    "v_cvt_pk_bf16_f32 v56, v56, v57\n\t"
    "v_cvt_pk_bf16_f32 v57, v58, v59\n\t"
    "v_cvt_pk_bf16_f32 v58, v60, v61\n\t"
    "v_cvt_pk_bf16_f32 v59, v62, v63\n\t"
    "ds_read_b64_tr_b16 v[160:161], v228 offset:0\n\t"
    "ds_read_b64_tr_b16 v[162:163], v228 offset:2048\n\t"
    "ds_read_b64_tr_b16 v[176:177], v228 offset:512\n\t"
    "ds_read_b64_tr_b16 v[178:179], v228 offset:2560\n\t"
    "ds_read_b64_tr_b16 v[192:193], v228 offset:1024\n\t"
    "ds_read_b64_tr_b16 v[194:195], v228 offset:3072\n\t"
    "ds_read_b64_tr_b16 v[208:209], v228 offset:1536\n\t"
    "ds_read_b64_tr_b16 v[210:211], v228 offset:3584\n\t"
    "ds_read_b64_tr_b16 v[164:165], v228 offset:4096\n\t"
    "ds_read_b64_tr_b16 v[166:167], v228 offset:6144\n\t"
    "ds_read_b64_tr_b16 v[180:181], v228 offset:4608\n\t"
    "ds_read_b64_tr_b16 v[182:183], v228 offset:6656\n\t"
    "ds_read_b64_tr_b16 v[196:197], v228 offset:5120\n\t"
    "ds_read_b64_tr_b16 v[198:199], v228 offset:7168\n\t"
    "ds_read_b64_tr_b16 v[212:213], v228 offset:5632\n\t"
    "ds_read_b64_tr_b16 v[214:215], v228 offset:7680\n\t"
    "ds_read_b64_tr_b16 v[168:169], v228 offset:8192\n\t"
    "ds_read_b64_tr_b16 v[170:171], v228 offset:10240\n\t"
    "ds_read_b64_tr_b16 v[184:185], v228 offset:8704\n\t"
    "ds_read_b64_tr_b16 v[186:187], v228 offset:10752\n\t"
    "ds_read_b64_tr_b16 v[200:201], v228 offset:9216\n\t"
    "ds_read_b64_tr_b16 v[202:203], v228 offset:11264\n\t"
    "ds_read_b64_tr_b16 v[216:217], v228 offset:9728\n\t"
    "ds_read_b64_tr_b16 v[218:219], v228 offset:11776\n\t"
    "ds_read_b64_tr_b16 v[172:173], v228 offset:12288\n\t"
    "ds_read_b64_tr_b16 v[174:175], v228 offset:14336\n\t"
    "ds_read_b64_tr_b16 v[188:189], v228 offset:12800\n\t"
    "ds_read_b64_tr_b16 v[190:191], v228 offset:14848\n\t"
    "ds_read_b64_tr_b16 v[204:205], v228 offset:13312\n\t"
    "ds_read_b64_tr_b16 v[206:207], v228 offset:15360\n\t"
    "ds_read_b64_tr_b16 v[220:221], v228 offset:13824\n\t"
    "ds_read_b64_tr_b16 v[222:223], v228 offset:15872\n\t"
    "s_waitcnt lgkmcnt(0)\n\t"
    "s_add_u32 s56, s56, 4\n\t"
    "s_addc_u32 s57, s57, 0\n\t"
    "s_setpc_b64 s[56:57]\n\t"
    "RARE_1_%=:\n\t"
    "s_sub_i32 s58, s50, %[skbias]\n\t"
    "s_add_i32 s59, s58, 8192\n\t"
    "buffer_load_dwordx4 v[160:163], v248, %[srdK], s58 offen offset:0\n\t"
    "buffer_load_dwordx4 v[164:167], v248, %[srdK], s58 offen offset:32\n\t"
    "buffer_load_dwordx4 v[168:171], v248, %[srdK], s58 offen offset:64\n\t"
    "buffer_load_dwordx4 v[172:175], v248, %[srdK], s58 offen offset:96\n\t"
    "buffer_load_dwordx4 v[176:179], v248, %[srdK], s58 offen offset:128\n\t"
    "buffer_load_dwordx4 v[180:183], v248, %[srdK], s58 offen offset:160\n\t"
    "buffer_load_dwordx4 v[184:187], v248, %[srdK], s58 offen offset:192\n\t"
    "buffer_load_dwordx4 v[188:191], v248, %[srdK], s58 offen offset:224\n\t"
    "buffer_load_dwordx4 v[192:195], v248, %[srdK], s59 offen offset:0\n\t"
    "buffer_load_dwordx4 v[196:199], v248, %[srdK], s59 offen offset:32\n\t"
    "buffer_load_dwordx4 v[200:203], v248, %[srdK], s59 offen offset:64\n\t"
    "buffer_load_dwordx4 v[204:207], v248, %[srdK], s59 offen offset:96\n\t"
    "buffer_load_dwordx4 v[208:211], v248, %[srdK], s59 offen offset:128\n\t"
    "buffer_load_dwordx4 v[212:215], v248, %[srdK], s59 offen offset:160\n\t"
    "buffer_load_dwordx4 v[216:219], v248, %[srdK], s59 offen offset:192\n\t"
    "buffer_load_dwordx4 v[220:223], v248, %[srdK], s59 offen offset:224\n\t"
    "s_waitcnt vmcnt(0)\n\t"
    "v_mfma_f32_32x32x16_bf16 v[64:79], v[160:163], a[128:131], v[128:143]\n\t"
    "v_mfma_f32_32x32x16_bf16 v[80:95], v[160:163], a[160:163], v[144:159]\n\t"
    "v_mfma_f32_32x32x16_bf16 v[64:79], v[164:167], a[132:135], v[64:79]\n\t"
    "v_mfma_f32_32x32x16_bf16 v[80:95], v[164:167], a[164:167], v[80:95]\n\t"
    "v_mfma_f32_32x32x16_bf16 v[64:79], v[168:171], a[136:139], v[64:79]\n\t"
    "v_mfma_f32_32x32x16_bf16 v[80:95], v[168:171], a[168:171], v[80:95]\n\t"
    "v_mfma_f32_32x32x16_bf16 v[64:79], v[172:175], a[140:143], v[64:79]\n\t"
    "v_mfma_f32_32x32x16_bf16 v[80:95], v[172:175], a[172:175], v[80:95]\n\t"
    "v_mfma_f32_32x32x16_bf16 v[64:79], v[176:179], a[144:147], v[64:79]\n\t"
    "v_mfma_f32_32x32x16_bf16 v[80:95], v[176:179], a[176:179], v[80:95]\n\t"
    "v_mfma_f32_32x32x16_bf16 v[64:79], v[180:183], a[148:151], v[64:79]\n\t"
    "v_mfma_f32_32x32x16_bf16 v[80:95], v[180:183], a[180:183], v[80:95]\n\t"
    "v_mfma_f32_32x32x16_bf16 v[64:79], v[184:187], a[152:155], v[64:79]\n\t"
    "v_mfma_f32_32x32x16_bf16 v[80:95], v[184:187], a[184:187], v[80:95]\n\t"
    "v_mfma_f32_32x32x16_bf16 v[64:79], v[188:191], a[156:159], v[64:79]\n\t"
    "v_mfma_f32_32x32x16_bf16 v[80:95], v[188:191], a[188:191], v[80:95]\n\t"
    "v_mfma_f32_32x32x16_bf16 v[96:111], v[192:195], a[128:131], v[128:143]\n\t"
    "v_mfma_f32_32x32x16_bf16 v[112:127], v[192:195], a[160:163], v[144:159]\n\t"
    "v_mfma_f32_32x32x16_bf16 v[96:111], v[196:199], a[132:135], v[96:111]\n\t"
    "v_mfma_f32_32x32x16_bf16 v[112:127], v[196:199], a[164:167], v[112:127]\n\t"
    "v_mfma_f32_32x32x16_bf16 v[96:111], v[200:203], a[136:139], v[96:111]\n\t"
    "v_mfma_f32_32x32x16_bf16 v[112:127], v[200:203], a[168:171], v[112:127]\n\t"
    "v_mfma_f32_32x32x16_bf16 v[96:111], v[204:207], a[140:143], v[96:111]\n\t"
    "v_mfma_f32_32x32x16_bf16 v[112:127], v[204:207], a[172:175], v[112:127]\n\t"
    "v_mfma_f32_32x32x16_bf16 v[96:111], v[208:211], a[144:147], v[96:111]\n\t"
    "v_mfma_f32_32x32x16_bf16 v[112:127], v[208:211], a[176:179], v[112:127]\n\t"
    "v_mfma_f32_32x32x16_bf16 v[96:111], v[212:215], a[148:151], v[96:111]\n\t"
    "v_mfma_f32_32x32x16_bf16 v[112:127], v[212:215], a[180:183], v[112:127]\n\t"
    "v_mfma_f32_32x32x16_bf16 v[96:111], v[216:219], a[152:155], v[96:111]\n\t"
    "v_mfma_f32_32x32x16_bf16 v[112:127], v[216:219], a[184:187], v[112:127]\n\t"
    "v_mfma_f32_32x32x16_bf16 v[96:111], v[220:223], a[156:159], v[96:111]\n\t"
    "v_mfma_f32_32x32x16_bf16 v[112:127], v[220:223], a[188:191], v[112:127]\n\t"
    "s_nop 7\n\t"
    "s_nop 7\n\t"
    "v_max3_f32 v242, v64, v65, v66\n\t"
    "v_max3_f32 v242, v242, v67, v68\n\t"
    "v_max3_f32 v242, v242, v69, v70\n\t"
    "v_max3_f32 v242, v242, v71, v72\n\t"
    "v_max3_f32 v242, v242, v73, v74\n\t"
    "v_max3_f32 v242, v242, v75, v76\n\t"
    "v_max3_f32 v242, v242, v77, v78\n\t"
    "v_max3_f32 v242, v242, v79, v96\n\t"
    "v_max3_f32 v242, v242, v97, v98\n\t"
    "v_max3_f32 v242, v242, v99, v100\n\t"
    "v_max3_f32 v242, v242, v101, v102\n\t"
    "v_max3_f32 v242, v242, v103, v104\n\t"
    "v_max3_f32 v242, v242, v105, v106\n\t"
    "v_max3_f32 v242, v242, v107, v108\n\t"
    "v_max3_f32 v242, v242, v109, v110\n\t"
    "v_max_f32_e32 v242, v242, v111\n\t"
    "v_mov_b32_e32 v243, v242\n\t"
    "s_nop 1\n\t"
    "v_permlane32_swap_b32_e32 v242, v243\n\t"
    "v_max_f32_e32 v242, v242, v243\n\t"
    "v_max_f32_e32 v242, 0, v242\n\t"
    "v_add_f32_e32 v240, v240, v242\n\t"
    "v_sub_f32_e32 v244, 0, v242\n\t"
    "v_exp_f32_e32 v244, v244\n\t"
    "v_sub_f32_e32 v64, v64, v242\n\t"
    "v_sub_f32_e32 v65, v65, v242\n\t"
    "v_sub_f32_e32 v66, v66, v242\n\t"
    "v_sub_f32_e32 v67, v67, v242\n\t"
    "v_sub_f32_e32 v68, v68, v242\n\t"
    "v_sub_f32_e32 v69, v69, v242\n\t"
    "v_sub_f32_e32 v70, v70, v242\n\t"
    "v_sub_f32_e32 v71, v71, v242\n\t"
    "v_sub_f32_e32 v72, v72, v242\n\t"
    "v_sub_f32_e32 v73, v73, v242\n\t"
    "v_sub_f32_e32 v74, v74, v242\n\t"
    "v_sub_f32_e32 v75, v75, v242\n\t"
    "v_sub_f32_e32 v76, v76, v242\n\t"
    "v_sub_f32_e32 v77, v77, v242\n\t"
    "v_sub_f32_e32 v78, v78, v242\n\t"
    "v_sub_f32_e32 v79, v79, v242\n\t"
    "v_sub_f32_e32 v96, v96, v242\n\t"
    "v_sub_f32_e32 v97, v97, v242\n\t"
    "v_sub_f32_e32 v98, v98, v242\n\t"
    "v_sub_f32_e32 v99, v99, v242\n\t"
    "v_sub_f32_e32 v100, v100, v242\n\t"
    "v_sub_f32_e32 v101, v101, v242\n\t"
    "v_sub_f32_e32 v102, v102, v242\n\t"
    "v_sub_f32_e32 v103, v103, v242\n\t"
    "v_sub_f32_e32 v104, v104, v242\n\t"
    "v_sub_f32_e32 v105, v105, v242\n\t"
    "v_sub_f32_e32 v106, v106, v242\n\t"
    "v_sub_f32_e32 v107, v107, v242\n\t"
    "v_sub_f32_e32 v108, v108, v242\n\t"
    "v_sub_f32_e32 v109, v109, v242\n\t"
    "v_sub_f32_e32 v110, v110, v242\n\t"
    "v_sub_f32_e32 v111, v111, v242\n\t"
    "v_sub_f32_e32 v0, v0, v242\n\t"
    "v_sub_f32_e32 v1, v1, v242\n\t"
    "v_sub_f32_e32 v2, v2, v242\n\t"
    "v_sub_f32_e32 v3, v3, v242\n\t"
    "v_sub_f32_e32 v4, v4, v242\n\t"
    "v_sub_f32_e32 v5, v5, v242\n\t"
    "v_sub_f32_e32 v6, v6, v242\n\t"
    "v_sub_f32_e32 v7, v7, v242\n\t"
    "v_sub_f32_e32 v8, v8, v242\n\t"
    "v_sub_f32_e32 v9, v9, v242\n\t"
    "v_sub_f32_e32 v10, v10, v242\n\t"
    "v_sub_f32_e32 v11, v11, v242\n\t"
    "v_sub_f32_e32 v12, v12, v242\n\t"
    "v_sub_f32_e32 v13, v13, v242\n\t"
    "v_sub_f32_e32 v14, v14, v242\n\t"
    "v_sub_f32_e32 v15, v15, v242\n\t"
    "v_sub_f32_e32 v32, v32, v242\n\t"
    "v_sub_f32_e32 v33, v33, v242\n\t"
    "v_sub_f32_e32 v34, v34, v242\n\t"
    "v_sub_f32_e32 v35, v35, v242\n\t"
    "v_sub_f32_e32 v36, v36, v242\n\t"
    "v_sub_f32_e32 v37, v37, v242\n\t"
    "v_sub_f32_e32 v38, v38, v242\n\t"
    "v_sub_f32_e32 v39, v39, v242\n\t"
    "v_sub_f32_e32 v40, v40, v242\n\t"
    "v_sub_f32_e32 v41, v41, v242\n\t"
    "v_sub_f32_e32 v42, v42, v242\n\t"
    "v_sub_f32_e32 v43, v43, v242\n\t"
    "v_sub_f32_e32 v44, v44, v242\n\t"
    "v_sub_f32_e32 v45, v45, v242\n\t"
    "v_sub_f32_e32 v46, v46, v242\n\t"
    "v_sub_f32_e32 v47, v47, v242\n\t"
    "v_sub_f32_e32 v243, 0, v240\n\t"
    "v_mov_b32_e32 v128, v243\n\t"
    "v_mov_b32_e32 v129, v243\n\t"
    "v_mov_b32_e32 v130, v243\n\t"
    "v_mov_b32_e32 v131, v243\n\t"
    "v_mov_b32_e32 v132, v243\n\t"
    "v_mov_b32_e32 v133, v243\n\t"
    "v_mov_b32_e32 v134, v243\n\t"
    "v_mov_b32_e32 v135, v243\n\t"
    "v_mov_b32_e32 v136, v243\n\t"
    "v_mov_b32_e32 v137, v243\n\t"
    "v_mov_b32_e32 v138, v243\n\t"
    "v_mov_b32_e32 v139, v243\n\t"
    "v_mov_b32_e32 v140, v243\n\t"
    "v_mov_b32_e32 v141, v243\n\t"
    "v_mov_b32_e32 v142, v243\n\t"
    "v_mov_b32_e32 v143, v243\n\t"
    "v_mul_f32_e32 v236, v236, v244\n\t"
    "v_mul_f32_e32 v237, v237, v244\n\t"
    "v_max3_f32 v242, v80, v81, v82\n\t"
    "v_max3_f32 v242, v242, v83, v84\n\t"
    "v_max3_f32 v242, v242, v85, v86\n\t"
    "v_max3_f32 v242, v242, v87, v88\n\t"
    "v_max3_f32 v242, v242, v89, v90\n\t"
    "v_max3_f32 v242, v242, v91, v92\n\t"
    "v_max3_f32 v242, v242, v93, v94\n\t"
    "v_max3_f32 v242, v242, v95, v112\n\t"
    "v_max3_f32 v242, v242, v113, v114\n\t"
    "v_max3_f32 v242, v242, v115, v116\n\t"
    "v_max3_f32 v242, v242, v117, v118\n\t"
    "v_max3_f32 v242, v242, v119, v120\n\t"
    "v_max3_f32 v242, v242, v121, v122\n\t"
    "v_max3_f32 v242, v242, v123, v124\n\t"
    "v_max3_f32 v242, v242, v125, v126\n\t"
    "v_max_f32_e32 v242, v242, v127\n\t"
    "v_mov_b32_e32 v243, v242\n\t"
    "s_nop 1\n\t"
    "v_permlane32_swap_b32_e32 v242, v243\n\t"
    "v_max_f32_e32 v242, v242, v243\n\t"
    "v_max_f32_e32 v242, 0, v242\n\t"
    "v_add_f32_e32 v241, v241, v242\n\t"
    "v_sub_f32_e32 v245, 0, v242\n\t"
    "v_exp_f32_e32 v245, v245\n\t"
    "v_sub_f32_e32 v80, v80, v242\n\t"
    "v_sub_f32_e32 v81, v81, v242\n\t"
    "v_sub_f32_e32 v82, v82, v242\n\t"
    "v_sub_f32_e32 v83, v83, v242\n\t"
    "v_sub_f32_e32 v84, v84, v242\n\t"
    "v_sub_f32_e32 v85, v85, v242\n\t"
    "v_sub_f32_e32 v86, v86, v242\n\t"
    "v_sub_f32_e32 v87, v87, v242\n\t"
    "v_sub_f32_e32 v88, v88, v242\n\t"
    "v_sub_f32_e32 v89, v89, v242\n\t"
    "v_sub_f32_e32 v90, v90, v242\n\t"
    "v_sub_f32_e32 v91, v91, v242\n\t"
    "v_sub_f32_e32 v92, v92, v242\n\t"
    "v_sub_f32_e32 v93, v93, v242\n\t"
    "v_sub_f32_e32 v94, v94, v242\n\t"
    "v_sub_f32_e32 v95, v95, v242\n\t"
    "v_sub_f32_e32 v112, v112, v242\n\t"
    "v_sub_f32_e32 v113, v113, v242\n\t"
    "v_sub_f32_e32 v114, v114, v242\n\t"
    "v_sub_f32_e32 v115, v115, v242\n\t"
    "v_sub_f32_e32 v116, v116, v242\n\t"
    "v_sub_f32_e32 v117, v117, v242\n\t"
    "v_sub_f32_e32 v118, v118, v242\n\t"
    "v_sub_f32_e32 v119, v119, v242\n\t"
    "v_sub_f32_e32 v120, v120, v242\n\t"
    "v_sub_f32_e32 v121, v121, v242\n\t"
    "v_sub_f32_e32 v122, v122, v242\n\t"
    "v_sub_f32_e32 v123, v123, v242\n\t"
    "v_sub_f32_e32 v124, v124, v242\n\t"
    "v_sub_f32_e32 v125, v125, v242\n\t"
    "v_sub_f32_e32 v126, v126, v242\n\t"
    "v_sub_f32_e32 v127, v127, v242\n\t"
    "v_sub_f32_e32 v16, v16, v242\n\t"
    "v_sub_f32_e32 v17, v17, v242\n\t"
    "v_sub_f32_e32 v18, v18, v242\n\t"
    "v_sub_f32_e32 v19, v19, v242\n\t"
    "v_sub_f32_e32 v20, v20, v242\n\t"
    "v_sub_f32_e32 v21, v21, v242\n\t"
    "v_sub_f32_e32 v22, v22, v242\n\t"
    "v_sub_f32_e32 v23, v23, v242\n\t"
    "v_sub_f32_e32 v24, v24, v242\n\t"
    "v_sub_f32_e32 v25, v25, v242\n\t"
    "v_sub_f32_e32 v26, v26, v242\n\t"
    "v_sub_f32_e32 v27, v27, v242\n\t"
    "v_sub_f32_e32 v28, v28, v242\n\t"
    "v_sub_f32_e32 v29, v29, v242\n\t"
    "v_sub_f32_e32 v30, v30, v242\n\t"
    "v_sub_f32_e32 v31, v31, v242\n\t"
    "v_sub_f32_e32 v48, v48, v242\n\t"
    "v_sub_f32_e32 v49, v49, v242\n\t"
    "v_sub_f32_e32 v50, v50, v242\n\t"
    "v_sub_f32_e32 v51, v51, v242\n\t"
    "v_sub_f32_e32 v52, v52, v242\n\t"
    "v_sub_f32_e32 v53, v53, v242\n\t"
    "v_sub_f32_e32 v54, v54, v242\n\t"
    "v_sub_f32_e32 v55, v55, v242\n\t"
    "v_sub_f32_e32 v56, v56, v242\n\t"
    "v_sub_f32_e32 v57, v57, v242\n\t"
    "v_sub_f32_e32 v58, v58, v242\n\t"
    "v_sub_f32_e32 v59, v59, v242\n\t"
    "v_sub_f32_e32 v60, v60, v242\n\t"
    "v_sub_f32_e32 v61, v61, v242\n\t"
    "v_sub_f32_e32 v62, v62, v242\n\t"
    "v_sub_f32_e32 v63, v63, v242\n\t"
    "v_sub_f32_e32 v243, 0, v241\n\t"
    "v_mov_b32_e32 v144, v243\n\t"
    "v_mov_b32_e32 v145, v243\n\t"
    "v_mov_b32_e32 v146, v243\n\t"
    "v_mov_b32_e32 v147, v243\n\t"
    "v_mov_b32_e32 v148, v243\n\t"
    "v_mov_b32_e32 v149, v243\n\t"
    "v_mov_b32_e32 v150, v243\n\t"
    "v_mov_b32_e32 v151, v243\n\t"
    "v_mov_b32_e32 v152, v243\n\t"
    "v_mov_b32_e32 v153, v243\n\t"
    "v_mov_b32_e32 v154, v243\n\t"
    "v_mov_b32_e32 v155, v243\n\t"
    "v_mov_b32_e32 v156, v243\n\t"
    "v_mov_b32_e32 v157, v243\n\t"
    "v_mov_b32_e32 v158, v243\n\t"
    "v_mov_b32_e32 v159, v243\n\t"
    "v_mul_f32_e32 v238, v238, v245\n\t"
    "v_mul_f32_e32 v239, v239, v245\n\t"
    "v_accvgpr_read_b32 v246, a0\n\t"
    "v_mul_f32_e32 v246, v246, v244\n\t"
    "v_accvgpr_write_b32 a0, v246\n\t"
    "v_accvgpr_read_b32 v246, a1\n\t"
    "v_mul_f32_e32 v246, v246, v244\n\t"
    "v_accvgpr_write_b32 a1, v246\n\t"
    "v_accvgpr_read_b32 v246, a2\n\t"
    "v_mul_f32_e32 v246, v246, v244\n\t"
    "v_accvgpr_write_b32 a2, v246\n\t"
    "v_accvgpr_read_b32 v246, a3\n\t"
    "v_mul_f32_e32 v246, v246, v244\n\t"
    "v_accvgpr_write_b32 a3, v246\n\t"
    "v_accvgpr_read_b32 v246, a4\n\t"
    "v_mul_f32_e32 v246, v246, v244\n\t"
    "v_accvgpr_write_b32 a4, v246\n\t"
    "v_accvgpr_read_b32 v246, a5\n\t"
    "v_mul_f32_e32 v246, v246, v244\n\t"
    "v_accvgpr_write_b32 a5, v246\n\t"
    "v_accvgpr_read_b32 v246, a6\n\t"
    "v_mul_f32_e32 v246, v246, v244\n\t"
    "v_accvgpr_write_b32 a6, v246\n\t"
    "v_accvgpr_read_b32 v246, a7\n\t"
    "v_mul_f32_e32 v246, v246, v244\n\t"
    "v_accvgpr_write_b32 a7, v246\n\t"
    "v_accvgpr_read_b32 v246, a8\n\t"
    "v_mul_f32_e32 v246, v246, v244\n\t"
    "v_accvgpr_write_b32 a8, v246\n\t"
    "v_accvgpr_read_b32 v246, a9\n\t"
    "v_mul_f32_e32 v246, v246, v244\n\t"
    "v_accvgpr_write_b32 a9, v246\n\t"
    "v_accvgpr_read_b32 v246, a10\n\t"
    "v_mul_f32_e32 v246, v246, v244\n\t"
    "v_accvgpr_write_b32 a10, v246\n\t"
    "v_accvgpr_read_b32 v246, a11\n\t"
    "v_mul_f32_e32 v246, v246, v244\n\t"
    "v_accvgpr_write_b32 a11, v246\n\t"
    "v_accvgpr_read_b32 v246, a12\n\t"
    "v_mul_f32_e32 v246, v246, v244\n\t"
    "v_accvgpr_write_b32 a12, v246\n\t"
    "v_accvgpr_read_b32 v246, a13\n\t"
    "v_mul_f32_e32 v246, v246, v244\n\t"
    "v_accvgpr_write_b32 a13, v246\n\t"
    "v_accvgpr_read_b32 v246, a14\n\t"
    "v_mul_f32_e32 v246, v246, v244\n\t"
    "v_accvgpr_write_b32 a14, v246\n\t"
    "v_accvgpr_read_b32 v246, a15\n\t"
    "v_mul_f32_e32 v246, v246, v244\n\t"
    "v_accvgpr_write_b32 a15, v246\n\t"
    "v_accvgpr_read_b32 v246, a16\n\t"
    "v_mul_f32_e32 v246, v246, v245\n\t"
    "v_accvgpr_write_b32 a16, v246\n\t"
    "v_accvgpr_read_b32 v246, a17\n\t"
    "v_mul_f32_e32 v246, v246, v245\n\t"
    "v_accvgpr_write_b32 a17, v246\n\t"
    "v_accvgpr_read_b32 v246, a18\n\t"
    "v_mul_f32_e32 v246, v246, v245\n\t"
    "v_accvgpr_write_b32 a18, v246\n\t"
    "v_accvgpr_read_b32 v246, a19\n\t"
    "v_mul_f32_e32 v246, v246, v245\n\t"
    "v_accvgpr_write_b32 a19, v246\n\t"
    "v_accvgpr_read_b32 v246, a20\n\t"
    "v_mul_f32_e32 v246, v246, v245\n\t"
    "v_accvgpr_write_b32 a20, v246\n\t"
    "v_accvgpr_read_b32 v246, a21\n\t"
    "v_mul_f32_e32 v246, v246, v245\n\t"
    "v_accvgpr_write_b32 a21, v246\n\t"
    "v_accvgpr_read_b32 v246, a22\n\t"
    "v_mul_f32_e32 v246, v246, v245\n\t"
    "v_accvgpr_write_b32 a22, v246\n\t"
    "v_accvgpr_read_b32 v246, a23\n\t"
    "v_mul_f32_e32 v246, v246, v245\n\t"
    "v_accvgpr_write_b32 a23, v246\n\t"
    "v_accvgpr_read_b32 v246, a24\n\t"
    "v_mul_f32_e32 v246, v246, v245\n\t"
    "v_accvgpr_write_b32 a24, v246\n\t"
    "v_accvgpr_read_b32 v246, a25\n\t"
    "v_mul_f32_e32 v246, v246, v245\n\t"
    "v_accvgpr_write_b32 a25, v246\n\t"
    "v_accvgpr_read_b32 v246, a26\n\t"
    "v_mul_f32_e32 v246, v246, v245\n\t"
    "v_accvgpr_write_b32 a26, v246\n\t"
    "v_accvgpr_read_b32 v246, a27\n\t"
    "v_mul_f32_e32 v246, v246, v245\n\t"
    "v_accvgpr_write_b32 a27, v246\n\t"
    "v_accvgpr_read_b32 v246, a28\n\t"
    "v_mul_f32_e32 v246, v246, v245\n\t"
    "v_accvgpr_write_b32 a28, v246\n\t"
    "v_accvgpr_read_b32 v246, a29\n\t"
    "v_mul_f32_e32 v246, v246, v245\n\t"
    "v_accvgpr_write_b32 a29, v246\n\t"
    "v_accvgpr_read_b32 v246, a30\n\t"
    "v_mul_f32_e32 v246, v246, v245\n\t"
    "v_accvgpr_write_b32 a30, v246\n\t"
    "v_accvgpr_read_b32 v246, a31\n\t"
    "v_mul_f32_e32 v246, v246, v245\n\t"
    "v_accvgpr_write_b32 a31, v246\n\t"
    "v_accvgpr_read_b32 v246, a32\n\t"
    "v_mul_f32_e32 v246, v246, v244\n\t"
    "v_accvgpr_write_b32 a32, v246\n\t"
    "v_accvgpr_read_b32 v246, a33\n\t"
    "v_mul_f32_e32 v246, v246, v244\n\t"
    "v_accvgpr_write_b32 a33, v246\n\t"
    "v_accvgpr_read_b32 v246, a34\n\t"
    "v_mul_f32_e32 v246, v246, v244\n\t"
    "v_accvgpr_write_b32 a34, v246\n\t"
    "v_accvgpr_read_b32 v246, a35\n\t"
    "v_mul_f32_e32 v246, v246, v244\n\t"
    "v_accvgpr_write_b32 a35, v246\n\t"
    "v_accvgpr_read_b32 v246, a36\n\t"
    "v_mul_f32_e32 v246, v246, v244\n\t"
    "v_accvgpr_write_b32 a36, v246\n\t"
    "v_accvgpr_read_b32 v246, a37\n\t"
    "v_mul_f32_e32 v246, v246, v244\n\t"
    "v_accvgpr_write_b32 a37, v246\n\t"
    "v_accvgpr_read_b32 v246, a38\n\t"
    "v_mul_f32_e32 v246, v246, v244\n\t"
    "v_accvgpr_write_b32 a38, v246\n\t"
    "v_accvgpr_read_b32 v246, a39\n\t"
    "v_mul_f32_e32 v246, v246, v244\n\t"
    "v_accvgpr_write_b32 a39, v246\n\t"
    "v_accvgpr_read_b32 v246, a40\n\t"
    "v_mul_f32_e32 v246, v246, v244\n\t"
    "v_accvgpr_write_b32 a40, v246\n\t"
    "v_accvgpr_read_b32 v246, a41\n\t"
    "v_mul_f32_e32 v246, v246, v244\n\t"
    "v_accvgpr_write_b32 a41, v246\n\t"
    "v_accvgpr_read_b32 v246, a42\n\t"
    "v_mul_f32_e32 v246, v246, v244\n\t"
    "v_accvgpr_write_b32 a42, v246\n\t"
    "v_accvgpr_read_b32 v246, a43\n\t"
    "v_mul_f32_e32 v246, v246, v244\n\t"
    "v_accvgpr_write_b32 a43, v246\n\t"
    "v_accvgpr_read_b32 v246, a44\n\t"
    "v_mul_f32_e32 v246, v246, v244\n\t"
    "v_accvgpr_write_b32 a44, v246\n\t"
    "v_accvgpr_read_b32 v246, a45\n\t"
    "v_mul_f32_e32 v246, v246, v244\n\t"
    "v_accvgpr_write_b32 a45, v246\n\t"
    "v_accvgpr_read_b32 v246, a46\n\t"
    "v_mul_f32_e32 v246, v246, v244\n\t"
    "v_accvgpr_write_b32 a46, v246\n\t"
    "v_accvgpr_read_b32 v246, a47\n\t"
    "v_mul_f32_e32 v246, v246, v244\n\t"
    "v_accvgpr_write_b32 a47, v246\n\t"
    "v_accvgpr_read_b32 v246, a48\n\t"
    "v_mul_f32_e32 v246, v246, v245\n\t"
    "v_accvgpr_write_b32 a48, v246\n\t"
    "v_accvgpr_read_b32 v246, a49\n\t"
    "v_mul_f32_e32 v246, v246, v245\n\t"
    "v_accvgpr_write_b32 a49, v246\n\t"
    "v_accvgpr_read_b32 v246, a50\n\t"
    "v_mul_f32_e32 v246, v246, v245\n\t"
    "v_accvgpr_write_b32 a50, v246\n\t"
    "v_accvgpr_read_b32 v246, a51\n\t"
    "v_mul_f32_e32 v246, v246, v245\n\t"
    "v_accvgpr_write_b32 a51, v246\n\t"
    "v_accvgpr_read_b32 v246, a52\n\t"
    "v_mul_f32_e32 v246, v246, v245\n\t"
    "v_accvgpr_write_b32 a52, v246\n\t"
    "v_accvgpr_read_b32 v246, a53\n\t"
    "v_mul_f32_e32 v246, v246, v245\n\t"
    "v_accvgpr_write_b32 a53, v246\n\t"
    "v_accvgpr_read_b32 v246, a54\n\t"
    "v_mul_f32_e32 v246, v246, v245\n\t"
    "v_accvgpr_write_b32 a54, v246\n\t"
    "v_accvgpr_read_b32 v246, a55\n\t"
    "v_mul_f32_e32 v246, v246, v245\n\t"
    "v_accvgpr_write_b32 a55, v246\n\t"
    "v_accvgpr_read_b32 v246, a56\n\t"
    "v_mul_f32_e32 v246, v246, v245\n\t"
    "v_accvgpr_write_b32 a56, v246\n\t"
    "v_accvgpr_read_b32 v246, a57\n\t"
    "v_mul_f32_e32 v246, v246, v245\n\t"
    "v_accvgpr_write_b32 a57, v246\n\t"
    "v_accvgpr_read_b32 v246, a58\n\t"
    "v_mul_f32_e32 v246, v246, v245\n\t"
    "v_accvgpr_write_b32 a58, v246\n\t"
    "v_accvgpr_read_b32 v246, a59\n\t"
    "v_mul_f32_e32 v246, v246, v245\n\t"
    "v_accvgpr_write_b32 a59, v246\n\t"
    "v_accvgpr_read_b32 v246, a60\n\t"
    "v_mul_f32_e32 v246, v246, v245\n\t"
    "v_accvgpr_write_b32 a60, v246\n\t"
    "v_accvgpr_read_b32 v246, a61\n\t"
    "v_mul_f32_e32 v246, v246, v245\n\t"
    "v_accvgpr_write_b32 a61, v246\n\t"
    "v_accvgpr_read_b32 v246, a62\n\t"
    "v_mul_f32_e32 v246, v246, v245\n\t"
    "v_accvgpr_write_b32 a62, v246\n\t"
    "v_accvgpr_read_b32 v246, a63\n\t"
    "v_mul_f32_e32 v246, v246, v245\n\t"
    "v_accvgpr_write_b32 a63, v246\n\t"
    "v_accvgpr_read_b32 v246, a64\n\t"
    "v_mul_f32_e32 v246, v246, v244\n\t"
    "v_accvgpr_write_b32 a64, v246\n\t"
    "v_accvgpr_read_b32 v246, a65\n\t"
    "v_mul_f32_e32 v246, v246, v244\n\t"
    "v_accvgpr_write_b32 a65, v246\n\t"
    "v_accvgpr_read_b32 v246, a66\n\t"
    "v_mul_f32_e32 v246, v246, v244\n\t"
    "v_accvgpr_write_b32 a66, v246\n\t"
    "v_accvgpr_read_b32 v246, a67\n\t"
    "v_mul_f32_e32 v246, v246, v244\n\t"
    "v_accvgpr_write_b32 a67, v246\n\t"
    "v_accvgpr_read_b32 v246, a68\n\t"
    "v_mul_f32_e32 v246, v246, v244\n\t"
    "v_accvgpr_write_b32 a68, v246\n\t"
    "v_accvgpr_read_b32 v246, a69\n\t"
    "v_mul_f32_e32 v246, v246, v244\n\t"
    "v_accvgpr_write_b32 a69, v246\n\t"
    "v_accvgpr_read_b32 v246, a70\n\t"
    "v_mul_f32_e32 v246, v246, v244\n\t"
    "v_accvgpr_write_b32 a70, v246\n\t"
    "v_accvgpr_read_b32 v246, a71\n\t"
    "v_mul_f32_e32 v246, v246, v244\n\t"
    "v_accvgpr_write_b32 a71, v246\n\t"
    "v_accvgpr_read_b32 v246, a72\n\t"
    "v_mul_f32_e32 v246, v246, v244\n\t"
    "v_accvgpr_write_b32 a72, v246\n\t"
    "v_accvgpr_read_b32 v246, a73\n\t"
    "v_mul_f32_e32 v246, v246, v244\n\t"
    "v_accvgpr_write_b32 a73, v246\n\t"
    "v_accvgpr_read_b32 v246, a74\n\t"
    "v_mul_f32_e32 v246, v246, v244\n\t"
    "v_accvgpr_write_b32 a74, v246\n\t"
    "v_accvgpr_read_b32 v246, a75\n\t"
    "v_mul_f32_e32 v246, v246, v244\n\t"
    "v_accvgpr_write_b32 a75, v246\n\t"
    "v_accvgpr_read_b32 v246, a76\n\t"
    "v_mul_f32_e32 v246, v246, v244\n\t"
    "v_accvgpr_write_b32 a76, v246\n\t"
    "v_accvgpr_read_b32 v246, a77\n\t"
    "v_mul_f32_e32 v246, v246, v244\n\t"
    "v_accvgpr_write_b32 a77, v246\n\t"
    "v_accvgpr_read_b32 v246, a78\n\t"
    "v_mul_f32_e32 v246, v246, v244\n\t"
    "v_accvgpr_write_b32 a78, v246\n\t"
    "v_accvgpr_read_b32 v246, a79\n\t"
    "v_mul_f32_e32 v246, v246, v244\n\t"
    "v_accvgpr_write_b32 a79, v246\n\t"
    "v_accvgpr_read_b32 v246, a80\n\t"
    "v_mul_f32_e32 v246, v246, v245\n\t"
    "v_accvgpr_write_b32 a80, v246\n\t"
    "v_accvgpr_read_b32 v246, a81\n\t"
    "v_mul_f32_e32 v246, v246, v245\n\t"
    "v_accvgpr_write_b32 a81, v246\n\t"
    "v_accvgpr_read_b32 v246, a82\n\t"
    "v_mul_f32_e32 v246, v246, v245\n\t"
    "v_accvgpr_write_b32 a82, v246\n\t"
    "v_accvgpr_read_b32 v246, a83\n\t"
    "v_mul_f32_e32 v246, v246, v245\n\t"
    "v_accvgpr_write_b32 a83, v246\n\t"
    "v_accvgpr_read_b32 v246, a84\n\t"
    "v_mul_f32_e32 v246, v246, v245\n\t"
    "v_accvgpr_write_b32 a84, v246\n\t"
    "v_accvgpr_read_b32 v246, a85\n\t"
    "v_mul_f32_e32 v246, v246, v245\n\t"
    "v_accvgpr_write_b32 a85, v246\n\t"
    "v_accvgpr_read_b32 v246, a86\n\t"
    "v_mul_f32_e32 v246, v246, v245\n\t"
    "v_accvgpr_write_b32 a86, v246\n\t"
    "v_accvgpr_read_b32 v246, a87\n\t"
    "v_mul_f32_e32 v246, v246, v245\n\t"
    "v_accvgpr_write_b32 a87, v246\n\t"
    "v_accvgpr_read_b32 v246, a88\n\t"
    "v_mul_f32_e32 v246, v246, v245\n\t"
    "v_accvgpr_write_b32 a88, v246\n\t"
    "v_accvgpr_read_b32 v246, a89\n\t"
    "v_mul_f32_e32 v246, v246, v245\n\t"
    "v_accvgpr_write_b32 a89, v246\n\t"
    "v_accvgpr_read_b32 v246, a90\n\t"
    "v_mul_f32_e32 v246, v246, v245\n\t"
    "v_accvgpr_write_b32 a90, v246\n\t"
    "v_accvgpr_read_b32 v246, a91\n\t"
    "v_mul_f32_e32 v246, v246, v245\n\t"
    "v_accvgpr_write_b32 a91, v246\n\t"
    "v_accvgpr_read_b32 v246, a92\n\t"
    "v_mul_f32_e32 v246, v246, v245\n\t"
    "v_accvgpr_write_b32 a92, v246\n\t"
    "v_accvgpr_read_b32 v246, a93\n\t"
    "v_mul_f32_e32 v246, v246, v245\n\t"
    "v_accvgpr_write_b32 a93, v246\n\t"
    "v_accvgpr_read_b32 v246, a94\n\t"
    "v_mul_f32_e32 v246, v246, v245\n\t"
    "v_accvgpr_write_b32 a94, v246\n\t"
    "v_accvgpr_read_b32 v246, a95\n\t"
    "v_mul_f32_e32 v246, v246, v245\n\t"
    "v_accvgpr_write_b32 a95, v246\n\t"
    "v_accvgpr_read_b32 v246, a96\n\t"
    "v_mul_f32_e32 v246, v246, v244\n\t"
    "v_accvgpr_write_b32 a96, v246\n\t"
    "v_accvgpr_read_b32 v246, a97\n\t"
    "v_mul_f32_e32 v246, v246, v244\n\t"
    "v_accvgpr_write_b32 a97, v246\n\t"
    "v_accvgpr_read_b32 v246, a98\n\t"
    "v_mul_f32_e32 v246, v246, v244\n\t"
    "v_accvgpr_write_b32 a98, v246\n\t"
    "v_accvgpr_read_b32 v246, a99\n\t"
    "v_mul_f32_e32 v246, v246, v244\n\t"
    "v_accvgpr_write_b32 a99, v246\n\t"
    "v_accvgpr_read_b32 v246, a100\n\t"
    "v_mul_f32_e32 v246, v246, v244\n\t"
    "v_accvgpr_write_b32 a100, v246\n\t"
    "v_accvgpr_read_b32 v246, a101\n\t"
    "v_mul_f32_e32 v246, v246, v244\n\t"
    "v_accvgpr_write_b32 a101, v246\n\t"
    "v_accvgpr_read_b32 v246, a102\n\t"
    "v_mul_f32_e32 v246, v246, v244\n\t"
    "v_accvgpr_write_b32 a102, v246\n\t"
    "v_accvgpr_read_b32 v246, a103\n\t"
    "v_mul_f32_e32 v246, v246, v244\n\t"
    "v_accvgpr_write_b32 a103, v246\n\t"
    "v_accvgpr_read_b32 v246, a104\n\t"
    "v_mul_f32_e32 v246, v246, v244\n\t"
    "v_accvgpr_write_b32 a104, v246\n\t"
    "v_accvgpr_read_b32 v246, a105\n\t"
    "v_mul_f32_e32 v246, v246, v244\n\t"
    "v_accvgpr_write_b32 a105, v246\n\t"
    "v_accvgpr_read_b32 v246, a106\n\t"
    "v_mul_f32_e32 v246, v246, v244\n\t"
    "v_accvgpr_write_b32 a106, v246\n\t"
    "v_accvgpr_read_b32 v246, a107\n\t"
    "v_mul_f32_e32 v246, v246, v244\n\t"
    "v_accvgpr_write_b32 a107, v246\n\t"
    "v_accvgpr_read_b32 v246, a108\n\t"
    "v_mul_f32_e32 v246, v246, v244\n\t"
    "v_accvgpr_write_b32 a108, v246\n\t"
    "v_accvgpr_read_b32 v246, a109\n\t"
    "v_mul_f32_e32 v246, v246, v244\n\t"
    "v_accvgpr_write_b32 a109, v246\n\t"
    "v_accvgpr_read_b32 v246, a110\n\t"
    "v_mul_f32_e32 v246, v246, v244\n\t"
    "v_accvgpr_write_b32 a110, v246\n\t"
    "v_accvgpr_read_b32 v246, a111\n\t"
    "v_mul_f32_e32 v246, v246, v244\n\t"
    "v_accvgpr_write_b32 a111, v246\n\t"
    "v_accvgpr_read_b32 v246, a112\n\t"
    "v_mul_f32_e32 v246, v246, v245\n\t"
    "v_accvgpr_write_b32 a112, v246\n\t"
    "v_accvgpr_read_b32 v246, a113\n\t"
    "v_mul_f32_e32 v246, v246, v245\n\t"
    "v_accvgpr_write_b32 a113, v246\n\t"
    "v_accvgpr_read_b32 v246, a114\n\t"
    "v_mul_f32_e32 v246, v246, v245\n\t"
    "v_accvgpr_write_b32 a114, v246\n\t"
    "v_accvgpr_read_b32 v246, a115\n\t"
    "v_mul_f32_e32 v246, v246, v245\n\t"
    "v_accvgpr_write_b32 a115, v246\n\t"
    "v_accvgpr_read_b32 v246, a116\n\t"
    "v_mul_f32_e32 v246, v246, v245\n\t"
    "v_accvgpr_write_b32 a116, v246\n\t"
    "v_accvgpr_read_b32 v246, a117\n\t"
    "v_mul_f32_e32 v246, v246, v245\n\t"
    "v_accvgpr_write_b32 a117, v246\n\t"
    "v_accvgpr_read_b32 v246, a118\n\t"
    "v_mul_f32_e32 v246, v246, v245\n\t"
    "v_accvgpr_write_b32 a118, v246\n\t"
    "v_accvgpr_read_b32 v246, a119\n\t"
    "v_mul_f32_e32 v246, v246, v245\n\t"
    "v_accvgpr_write_b32 a119, v246\n\t"
    "v_accvgpr_read_b32 v246, a120\n\t"
    "v_mul_f32_e32 v246, v246, v245\n\t"
    "v_accvgpr_write_b32 a120, v246\n\t"
    "v_accvgpr_read_b32 v246, a121\n\t"
    "v_mul_f32_e32 v246, v246, v245\n\t"
    "v_accvgpr_write_b32 a121, v246\n\t"
    "v_accvgpr_read_b32 v246, a122\n\t"
    "v_mul_f32_e32 v246, v246, v245\n\t"
    "v_accvgpr_write_b32 a122, v246\n\t"
    "v_accvgpr_read_b32 v246, a123\n\t"
    "v_mul_f32_e32 v246, v246, v245\n\t"
    "v_accvgpr_write_b32 a123, v246\n\t"
    "v_accvgpr_read_b32 v246, a124\n\t"
    "v_mul_f32_e32 v246, v246, v245\n\t"
    "v_accvgpr_write_b32 a124, v246\n\t"
    "v_accvgpr_read_b32 v246, a125\n\t"
    "v_mul_f32_e32 v246, v246, v245\n\t"
    "v_accvgpr_write_b32 a125, v246\n\t"
    "v_accvgpr_read_b32 v246, a126\n\t"
    "v_mul_f32_e32 v246, v246, v245\n\t"
    "v_accvgpr_write_b32 a126, v246\n\t"
    "v_accvgpr_read_b32 v246, a127\n\t"
    "v_mul_f32_e32 v246, v246, v245\n\t"
    "v_accvgpr_write_b32 a127, v246\n\t"
    "v_exp_f32_e32 v64, v64\n\t"
    "s_nop 0\n\t"
    "v_exp_f32_e32 v65, v65\n\t"
    "s_nop 0\n\t"
    "v_exp_f32_e32 v66, v66\n\t"
    "s_nop 0\n\t"
    "v_add_f32_e32 v232, v64, v66\n\t"
    "v_exp_f32_e32 v67, v67\n\t"
    "s_nop 0\n\t"
    "v_add_f32_e32 v233, v65, v67\n\t"
    "v_exp_f32_e32 v68, v68\n\t"
    "s_nop 0\n\t"
    "v_add_f32_e32 v232, v232, v68\n\t"
    "v_exp_f32_e32 v69, v69\n\t"
    "s_nop 0\n\t"
    "v_add_f32_e32 v233, v233, v69\n\t"
    "v_exp_f32_e32 v70, v70\n\t"
    "s_nop 0\n\t"
    "v_add_f32_e32 v232, v232, v70\n\t"
    "v_exp_f32_e32 v71, v71\n\t"
    "s_nop 0\n\t"
    "v_add_f32_e32 v233, v233, v71\n\t"
    "v_exp_f32_e32 v80, v80\n\t"
    "s_nop 0\n\t"
    "v_exp_f32_e32 v81, v81\n\t"
    "s_nop 0\n\t"
    "v_exp_f32_e32 v82, v82\n\t"
    "s_nop 0\n\t"
    "v_add_f32_e32 v234, v80, v82\n\t"
    "v_exp_f32_e32 v83, v83\n\t"
    "s_nop 0\n\t"
    "v_add_f32_e32 v235, v81, v83\n\t"
    "v_exp_f32_e32 v84, v84\n\t"
    "s_nop 0\n\t"
    "v_add_f32_e32 v234, v234, v84\n\t"
    "v_exp_f32_e32 v85, v85\n\t"
    "s_nop 0\n\t"
    "v_add_f32_e32 v235, v235, v85\n\t"
    "v_exp_f32_e32 v86, v86\n\t"
    "s_nop 0\n\t"
    "v_add_f32_e32 v234, v234, v86\n\t"
    "v_exp_f32_e32 v87, v87\n\t"
    "s_nop 0\n\t"
    "v_add_f32_e32 v235, v235, v87\n\t"
    "v_exp_f32_e32 v72, v72\n\t"
    "s_nop 0\n\t"
    "v_add_f32_e32 v232, v232, v72\n\t"
    "v_exp_f32_e32 v73, v73\n\t"
    "s_nop 0\n\t"
    "v_add_f32_e32 v233, v233, v73\n\t"
    "v_exp_f32_e32 v74, v74\n\t"
    "s_nop 0\n\t"
    "v_add_f32_e32 v232, v232, v74\n\t"
    "v_exp_f32_e32 v75, v75\n\t"
    "s_nop 0\n\t"
    "v_add_f32_e32 v233, v233, v75\n\t"
    "v_exp_f32_e32 v76, v76\n\t"
    "s_nop 0\n\t"
    "v_add_f32_e32 v232, v232, v76\n\t"
    "v_exp_f32_e32 v77, v77\n\t"
    "s_nop 0\n\t"
    "v_add_f32_e32 v233, v233, v77\n\t"
    "v_exp_f32_e32 v78, v78\n\t"
    "s_nop 0\n\t"
    "v_add_f32_e32 v232, v232, v78\n\t"
    "v_exp_f32_e32 v79, v79\n\t"
    "s_nop 0\n\t"
    "v_add_f32_e32 v233, v233, v79\n\t"
    "v_exp_f32_e32 v88, v88\n\t"
    "s_nop 0\n\t"
    "v_add_f32_e32 v234, v234, v88\n\t"
    "v_exp_f32_e32 v89, v89\n\t"
    "s_nop 0\n\t"
    "v_add_f32_e32 v235, v235, v89\n\t"
    "v_exp_f32_e32 v90, v90\n\t"
    "s_nop 0\n\t"
    "v_add_f32_e32 v234, v234, v90\n\t"
    "v_exp_f32_e32 v91, v91\n\t"
    "s_nop 0\n\t"
    "v_add_f32_e32 v235, v235, v91\n\t"
    "v_exp_f32_e32 v92, v92\n\t"
    "s_nop 0\n\t"
    "v_add_f32_e32 v234, v234, v92\n\t"
    "v_exp_f32_e32 v93, v93\n\t"
    "s_nop 0\n\t"
    "v_add_f32_e32 v235, v235, v93\n\t"
    "v_exp_f32_e32 v94, v94\n\t"
    "s_nop 0\n\t"
    "v_add_f32_e32 v234, v234, v94\n\t"
    "v_exp_f32_e32 v95, v95\n\t"
    "s_nop 0\n\t"
    "v_add_f32_e32 v235, v235, v95\n\t"
    "v_exp_f32_e32 v96, v96\n\t"
    "s_nop 0\n\t"
    "v_add_f32_e32 v232, v232, v96\n\t"
    "v_exp_f32_e32 v97, v97\n\t"
    "s_nop 0\n\t"
    "v_add_f32_e32 v233, v233, v97\n\t"
    "v_exp_f32_e32 v98, v98\n\t"
    "s_nop 0\n\t"
    "v_add_f32_e32 v232, v232, v98\n\t"
    "v_exp_f32_e32 v99, v99\n\t"
    "s_nop 0\n\t"
    "v_add_f32_e32 v233, v233, v99\n\t"
    "v_exp_f32_e32 v100, v100\n\t"
    "s_nop 0\n\t"
    "v_add_f32_e32 v232, v232, v100\n\t"
    "v_exp_f32_e32 v101, v101\n\t"
    "s_nop 0\n\t"
    "v_add_f32_e32 v233, v233, v101\n\t"
    "v_exp_f32_e32 v102, v102\n\t"
    "s_nop 0\n\t"
    "v_add_f32_e32 v232, v232, v102\n\t"
    "v_exp_f32_e32 v103, v103\n\t"
    "s_nop 0\n\t"
    "v_add_f32_e32 v233, v233, v103\n\t"
    "v_exp_f32_e32 v112, v112\n\t"
    "s_nop 0\n\t"
    "v_add_f32_e32 v234, v234, v112\n\t"
    "v_exp_f32_e32 v113, v113\n\t"
    "s_nop 0\n\t"
    "v_add_f32_e32 v235, v235, v113\n\t"
    "v_exp_f32_e32 v114, v114\n\t"
    "s_nop 0\n\t"
    "v_add_f32_e32 v234, v234, v114\n\t"
    "v_exp_f32_e32 v115, v115\n\t"
    "s_nop 0\n\t"
    "v_add_f32_e32 v235, v235, v115\n\t"
    "v_exp_f32_e32 v116, v116\n\t"
    "s_nop 0\n\t"
    "v_add_f32_e32 v234, v234, v116\n\t"
    "v_exp_f32_e32 v117, v117\n\t"
    "s_nop 0\n\t"
    "v_add_f32_e32 v235, v235, v117\n\t"
    "v_exp_f32_e32 v118, v118\n\t"
    "s_nop 0\n\t"
    "v_add_f32_e32 v234, v234, v118\n\t"
    "v_exp_f32_e32 v119, v119\n\t"
    "s_nop 0\n\t"
    "v_add_f32_e32 v235, v235, v119\n\t"
    "v_exp_f32_e32 v104, v104\n\t"
    "s_nop 0\n\t"
    "v_add_f32_e32 v232, v232, v104\n\t"
    "v_exp_f32_e32 v105, v105\n\t"
    "s_nop 0\n\t"
    "v_add_f32_e32 v233, v233, v105\n\t"
    "v_exp_f32_e32 v106, v106\n\t"
    "s_nop 0\n\t"
    "v_add_f32_e32 v232, v232, v106\n\t"
    "v_exp_f32_e32 v107, v107\n\t"
    "s_nop 0\n\t"
    "v_add_f32_e32 v233, v233, v107\n\t"
    "v_exp_f32_e32 v108, v108\n\t"
    "s_nop 0\n\t"
    "v_add_f32_e32 v232, v232, v108\n\t"
    "v_exp_f32_e32 v109, v109\n\t"
    "s_nop 0\n\t"
    "v_add_f32_e32 v233, v233, v109\n\t"
    "v_exp_f32_e32 v110, v110\n\t"
    "s_nop 0\n\t"
    "v_add_f32_e32 v232, v232, v110\n\t"
    "v_exp_f32_e32 v111, v111\n\t"
    "s_nop 0\n\t"
    "v_add_f32_e32 v233, v233, v111\n\t"
    "v_exp_f32_e32 v120, v120\n\t"
    "s_nop 0\n\t"
    "v_add_f32_e32 v234, v234, v120\n\t"
    "v_exp_f32_e32 v121, v121\n\t"
    "s_nop 0\n\t"
    "v_add_f32_e32 v235, v235, v121\n\t"
    "v_exp_f32_e32 v122, v122\n\t"
    "s_nop 0\n\t"
    "v_add_f32_e32 v234, v234, v122\n\t"
    "v_exp_f32_e32 v123, v123\n\t"
    "s_nop 0\n\t"
    "v_add_f32_e32 v235, v235, v123\n\t"
    "v_exp_f32_e32 v124, v124\n\t"
    "s_nop 0\n\t"
    "v_add_f32_e32 v234, v234, v124\n\t"
    "v_exp_f32_e32 v125, v125\n\t"
    "s_nop 0\n\t"
    "v_add_f32_e32 v235, v235, v125\n\t"
    "v_exp_f32_e32 v126, v126\n\t"
    "s_nop 0\n\t"
    "v_add_f32_e32 v234, v234, v126\n\t"
    "v_exp_f32_e32 v127, v127\n\t"
    "s_nop 0\n\t"
    "v_add_f32_e32 v235, v235, v127\n\t"
    "v_cvt_pk_bf16_f32 v64, v64, v65\n\t"
    "v_cvt_pk_bf16_f32 v65, v66, v67\n\t"
    "v_cvt_pk_bf16_f32 v66, v68, v69\n\t"
    "v_cvt_pk_bf16_f32 v67, v70, v71\n\t"
    "v_cvt_pk_bf16_f32 v80, v80, v81\n\t"
    "v_cvt_pk_bf16_f32 v81, v82, v83\n\t"
    "v_cvt_pk_bf16_f32 v82, v84, v85\n\t"
    "v_cvt_pk_bf16_f32 v83, v86, v87\n\t"
    "v_cvt_pk_bf16_f32 v72, v72, v73\n\t"
    "v_cvt_pk_bf16_f32 v73, v74, v75\n\t"
    "v_cvt_pk_bf16_f32 v74, v76, v77\n\t"
    "v_cvt_pk_bf16_f32 v75, v78, v79\n\t"
    "v_cvt_pk_bf16_f32 v88, v88, v89\n\t"
    "v_cvt_pk_bf16_f32 v89, v90, v91\n\t"
    "v_cvt_pk_bf16_f32 v90, v92, v93\n\t"
    "v_cvt_pk_bf16_f32 v91, v94, v95\n\t"
    "v_cvt_pk_bf16_f32 v96, v96, v97\n\t"
    "v_cvt_pk_bf16_f32 v97, v98, v99\n\t"
    "v_cvt_pk_bf16_f32 v98, v100, v101\n\t"
    "v_cvt_pk_bf16_f32 v99, v102, v103\n\t"
    "v_cvt_pk_bf16_f32 v112, v112, v113\n\t"
    "v_cvt_pk_bf16_f32 v113, v114, v115\n\t"
    "v_cvt_pk_bf16_f32 v114, v116, v117\n\t"
    "v_cvt_pk_bf16_f32 v115, v118, v119\n\t"
    "v_cvt_pk_bf16_f32 v104, v104, v105\n\t"
    "v_cvt_pk_bf16_f32 v105, v106, v107\n\t"
    "v_cvt_pk_bf16_f32 v106, v108, v109\n\t"
    "v_cvt_pk_bf16_f32 v107, v110, v111\n\t"
    "v_cvt_pk_bf16_f32 v120, v120, v121\n\t"
    "v_cvt_pk_bf16_f32 v121, v122, v123\n\t"
    "v_cvt_pk_bf16_f32 v122, v124, v125\n\t"
    "v_cvt_pk_bf16_f32 v123, v126, v127\n\t"
    "ds_read_b64_tr_b16 v[160:161], v228 offset:16384\n\t"
    "ds_read_b64_tr_b16 v[162:163], v228 offset:18432\n\t"
    "ds_read_b64_tr_b16 v[176:177], v228 offset:16896\n\t"
    "ds_read_b64_tr_b16 v[178:179], v228 offset:18944\n\t"
    "ds_read_b64_tr_b16 v[192:193], v228 offset:17408\n\t"
    "ds_read_b64_tr_b16 v[194:195], v228 offset:19456\n\t"
    "ds_read_b64_tr_b16 v[208:209], v228 offset:17920\n\t"
    "ds_read_b64_tr_b16 v[210:211], v228 offset:19968\n\t"
    "ds_read_b64_tr_b16 v[164:165], v228 offset:20480\n\t"
    "ds_read_b64_tr_b16 v[166:167], v228 offset:22528\n\t"
    "ds_read_b64_tr_b16 v[180:181], v228 offset:20992\n\t"
    "ds_read_b64_tr_b16 v[182:183], v228 offset:23040\n\t"
    "ds_read_b64_tr_b16 v[196:197], v228 offset:21504\n\t"
    "ds_read_b64_tr_b16 v[198:199], v228 offset:23552\n\t"
    "ds_read_b64_tr_b16 v[212:213], v228 offset:22016\n\t"
    "ds_read_b64_tr_b16 v[214:215], v228 offset:24064\n\t"
    "ds_read_b64_tr_b16 v[168:169], v228 offset:24576\n\t"
    "ds_read_b64_tr_b16 v[170:171], v228 offset:26624\n\t"
    "ds_read_b64_tr_b16 v[184:185], v228 offset:25088\n\t"
    "ds_read_b64_tr_b16 v[186:187], v228 offset:27136\n\t"
    "ds_read_b64_tr_b16 v[200:201], v228 offset:25600\n\t"
    "ds_read_b64_tr_b16 v[202:203], v228 offset:27648\n\t"
    "ds_read_b64_tr_b16 v[216:217], v228 offset:26112\n\t"
    "ds_read_b64_tr_b16 v[218:219], v228 offset:28160\n\t"
    "ds_read_b64_tr_b16 v[172:173], v228 offset:28672\n\t"
    "ds_read_b64_tr_b16 v[174:175], v228 offset:30720\n\t"
    "ds_read_b64_tr_b16 v[188:189], v228 offset:29184\n\t"
    "ds_read_b64_tr_b16 v[190:191], v228 offset:31232\n\t"
    "ds_read_b64_tr_b16 v[204:205], v228 offset:29696\n\t"
    "ds_read_b64_tr_b16 v[206:207], v228 offset:31744\n\t"
    "ds_read_b64_tr_b16 v[220:221], v228 offset:30208\n\t"
    "ds_read_b64_tr_b16 v[222:223], v228 offset:32256\n\t"
    "s_waitcnt lgkmcnt(0)\n\t"
    "s_add_u32 s56, s56, 4\n\t"
    "s_addc_u32 s57, s57, 0\n\t"
    "s_setpc_b64 s[56:57]\n\t"
    "END_%=:\n\t"
    :
    : [vlane]"v"(lane), [ldsb]"s"(ldsb), [ostg]"s"(ostg),
      [srdQ]"s"(srdQ), [srdK]"s"(srdK), [srdV]"s"(srdV), [srdO]"s"(srdO), [m0k]"s"(m0k), [m0v]"s"(m0v), [m0q]"s"(m0q), [sofk]"s"(sofk), [sofq]"s"(sofq),
      [qoff]"s"(qoff), [skbias]"s"(skbias), [soo]"s"(sofq), [soo1]"s"(sofq + 4096u), [soo2]"s"(sofq + 8192u), [soo3]"s"(sofq + 12288u)
    : "memory", "m0", "vcc", "scc", "s40","s41","s42","s43","s44","s45","s46","s47","s48","s49","s50","s51","s52","s53","s54","s55","s56","s57","s58","s59", "v0","v1","v2","v3","v4","v5","v6","v7","v8","v9","v10","v11","v12","v13","v14","v15","v16","v17","v18","v19","v20","v21","v22","v23","v24","v25","v26","v27","v28","v29","v30","v31","v32","v33","v34","v35","v36","v37","v38","v39","v40","v41","v42","v43","v44","v45","v46","v47","v48","v49","v50","v51","v52","v53","v54","v55","v56","v57","v58","v59","v60","v61","v62","v63","v64","v65","v66","v67","v68","v69","v70","v71","v72","v73","v74","v75","v76","v77","v78","v79","v80","v81","v82","v83","v84","v85","v86","v87","v88","v89","v90","v91","v92","v93","v94","v95","v96","v97","v98","v99","v100","v101","v102","v103","v104","v105","v106","v107","v108","v109","v110","v111","v112","v113","v114","v115","v116","v117","v118","v119","v120","v121","v122","v123","v124","v125","v126","v127","v128","v129","v130","v131","v132","v133","v134","v135","v136","v137","v138","v139","v140","v141","v142","v143","v144","v145","v146","v147","v148","v149","v150","v151","v152","v153","v154","v155","v156","v157","v158","v159","v160","v161","v162","v163","v164","v165","v166","v167","v168","v169","v170","v171","v172","v173","v174","v175","v176","v177","v178","v179","v180","v181","v182","v183","v184","v185","v186","v187","v188","v189","v190","v191","v192","v193","v194","v195","v196","v197","v198","v199","v200","v201","v202","v203","v204","v205","v206","v207","v208","v209","v210","v211","v212","v213","v214","v215","v216","v217","v218","v219","v220","v221","v222","v223","v224","v225","v226","v227","v228","v229","v230","v231","v232","v233","v234","v235","v236","v237","v238","v239","v240","v241","v242","v243","v244","v245","v246","v247","v248","v249", "a0","a1","a2","a3","a4","a5","a6","a7","a8","a9","a10","a11","a12","a13","a14","a15","a16","a17","a18","a19","a20","a21","a22","a23","a24","a25","a26","a27","a28","a29","a30","a31","a32","a33","a34","a35","a36","a37","a38","a39","a40","a41","a42","a43","a44","a45","a46","a47","a48","a49","a50","a51","a52","a53","a54","a55","a56","a57","a58","a59","a60","a61","a62","a63","a64","a65","a66","a67","a68","a69","a70","a71","a72","a73","a74","a75","a76","a77","a78","a79","a80","a81","a82","a83","a84","a85","a86","a87","a88","a89","a90","a91","a92","a93","a94","a95","a96","a97","a98","a99","a100","a101","a102","a103","a104","a105","a106","a107","a108","a109","a110","a111","a112","a113","a114","a115","a116","a117","a118","a119","a120","a121","a122","a123","a124","a125","a126","a127","a128","a129","a130","a131","a132","a133","a134","a135","a136","a137","a138","a139","a140","a141","a142","a143","a144","a145","a146","a147","a148","a149","a150","a151","a152","a153","a154","a155","a156","a157","a158","a159","a160","a161","a162","a163","a164","a165","a166","a167","a168","a169","a170","a171","a172","a173","a174","a175","a176","a177","a178","a179","a180","a181","a182","a183","a184","a185","a186","a187","a188","a189","a190","a191","a192","a193","a194","a195","a196","a197","a198","a199","a200","a201","a202","a203","a204","a205","a206","a207","a208","a209","a210","a211","a212","a213","a214","a215","a216","a217","a218","a219","a220","a221","a222","a223","a224","a225","a226","a227","a228","a229","a230","a231","a232","a233","a234","a235","a236","a237","a238","a239","a240","a241","a242","a243","a244","a245","a246","a247","a248","a249","a250","a251","a252","a253","a254","a255");

}
extern "C" void kernel_launch(void* const* d_in, const int* in_sizes, int n_in, void* d_out, int out_size, void* d_ws, size_t ws_size, hipStream_t stream) {
  hipLaunchKernelGGL(attn_fwd_asm, dim3(256), dim3(256), 0, stream, (const unsigned short*)d_in[0], (const unsigned short*)d_in[1], (const unsigned short*)d_in[2], (unsigned short*)d_out);
}
```
